```python
import torch

def _softmax(x, axis=-1):
    return torch.softmax(x, dim=axis)
def _gelu(x):
    return torch.nn.functional.gelu(x, approximate="tanh")
def _silu(x):
    return torch.nn.functional.silu(x)
def _sigmoid(x):
    return torch.sigmoid(x)
def _layernorm(x, axis=-1, eps=1e-5):
    m = x.mean(dim=axis, keepdim=True)
    v = x.var(dim=axis, keepdim=True, unbiased=False)
    return (x - m) / torch.sqrt(v + eps)
def _scatter_add(tgt, idx, src, axis=0):
    return tgt.clone().index_add_(axis, idx, src)
def _segment_sum(src, seg, n):
    out = torch.zeros((n,) + src.shape[1:], dtype=src.dtype)
    return out.index_add_(0, seg, src)
def _pad(x, pw):
    flat = tuple(v for pair in reversed(pw) for v in pair)
    return torch.nn.functional.pad(x, flat)

kernel_name = 'standalone__softmax_full_bidir__decode'

def setup_inputs():

    _w = (lambda a: (lambda t: t.to(torch.bfloat16) if t.is_floating_point() else t)(torch.as_tensor(a)))
    B, H, Sq, Skv, Dh = 4, 32, 1, 4096, 128
    return {
        "q": _w((lambda *s: torch.randn(*s, dtype=torch.bfloat16) * 0.8)(B, H, Sq, Dh)),
        "k": _w((lambda *s: torch.randn(*s, dtype=torch.bfloat16) * 0.8)(B, H, Skv, Dh)),
        "v": _w((lambda *s: torch.randn(*s, dtype=torch.bfloat16) * 0.05)(B, H, Skv, Dh)),
    }

def reference(q, k, v):
    scale = q.shape[-1] ** -0.5
    scores = torch.einsum('bhqd,bhkd->bhqk', q, k) * scale
    attn = _softmax(scores, axis=-1)
    return torch.einsum('bhqk,bhkd->bhqd', attn, v)
```

```cpp
#include <hip/hip_runtime.h>
#include <hip/hip_bf16.h>
#include <stdint.h>

constexpr int BH = 128, SKV = 4096, D = 128;
#define NSPLIT 4
#define NWAVE 4
#define U 4
#define RING 2
#define AUX 2
constexpr int KEYS_WG = SKV / NSPLIT;
constexpr int STEPS = KEYS_WG / (NWAVE * 4 * U);
static_assert(STEPS % RING == 0, "");

typedef unsigned u32x4 __attribute__((ext_vector_type(4)));
typedef __bf16 bf16x2 __attribute__((ext_vector_type(2)));
typedef float f32x4 __attribute__((ext_vector_type(4)));

__device__ __forceinline__ float dot2(unsigned a, unsigned b, float acc) {
  return __builtin_amdgcn_fdot2_f32_bf16(__builtin_bit_cast(bf16x2, a), __builtin_bit_cast(bf16x2, b), acc, false);
}
template <int CTRL> __device__ __forceinline__ float dpp(float x) {
  return __builtin_bit_cast(float, __builtin_amdgcn_mov_dpp(__builtin_bit_cast(int, x), CTRL, 0xf, 0xf, true));
}
__device__ __forceinline__ float row_allsum(float x) {
  x += dpp<0x128>(x); x += dpp<0x124>(x); x += dpp<0x122>(x); x += dpp<0x121>(x);
  return x;
}

__global__ void __launch_bounds__(NWAVE * 64) attn_part(const uint16_t* __restrict__ q, const uint16_t* __restrict__ k,
                                                        const uint16_t* __restrict__ v, float* __restrict__ ws) {
  __shared__ float lds[NWAVE * 4 * 132];
  const int bid = blockIdx.x;
  const int bh = bid / NSPLIT, sp = bid % NSPLIT;
  const int tid = threadIdx.x, lane = tid & 63, wave = __builtin_amdgcn_readfirstlane(tid >> 6);
  const int r = lane >> 4, c = lane & 15;
  constexpr float C2 = 0.088388347648318440f * 1.4426950408889634f;
  const size_t base = ((size_t)bh * SKV + (size_t)sp * KEYS_WG) * D;
  auto rk = __builtin_amdgcn_make_buffer_rsrc((void*)(k + base), 0, KEYS_WG * D * 2, 0x00020000);
  auto rv = __builtin_amdgcn_make_buffer_rsrc((void*)(v + base), 0, KEYS_WG * D * 2, 0x00020000);
  const int voff = lane * 16;
  u32x4 Kr[RING][U], Vr[RING][U];
#pragma unroll
  for (int g = 0; g < RING; ++g) {
    const int so = (g * NWAVE + wave) * U * 1024;
#pragma unroll
    for (int u = 0; u < U; ++u) Kr[g][u] = __builtin_amdgcn_raw_buffer_load_b128(rk, voff + u * 1024, so, AUX);
#pragma unroll
    for (int u = 0; u < U; ++u) Vr[g][u] = __builtin_amdgcn_raw_buffer_load_b128(rv, voff + u * 1024, so, AUX);
  }
  const u32x4 qv = *reinterpret_cast<const u32x4*>(q + (size_t)bh * D + 8 * c);
  float m = -INFINITY, l = 0.f, acc[8];
#pragma unroll
  for (int i = 0; i < 8; ++i) acc[i] = 0.f;
  for (int s0 = 0; s0 < STEPS; s0 += RING) {
#pragma unroll
    for (int g = 0; g < RING; ++g) {
      const int so = ((s0 + g + RING) * NWAVE + wave) * U * 1024;
      float sc[U];
#pragma unroll
      for (int u = 0; u < U; ++u) {
        float d = dot2(Kr[g][u][0], qv[0], 0.f);
        d = dot2(Kr[g][u][1], qv[1], d); d = dot2(Kr[g][u][2], qv[2], d); d = dot2(Kr[g][u][3], qv[3], d);
        sc[u] = d;
      }
#pragma unroll
      for (int u = 0; u < U; ++u) Kr[g][u] = __builtin_amdgcn_raw_buffer_load_b128(rk, voff + u * 1024, so, AUX);
#pragma unroll
      for (int u = 0; u < U; ++u) sc[u] = row_allsum(sc[u]) * C2;
      float mn = m;
#pragma unroll
      for (int u = 0; u < U; ++u) mn = fmaxf(mn, sc[u]);
      const float alpha = __builtin_amdgcn_exp2f(m - mn);
      m = mn;
      float p[U], ps = 0.f;
#pragma unroll
      for (int u = 0; u < U; ++u) { p[u] = __builtin_amdgcn_exp2f(sc[u] - mn); ps += p[u]; }
      l = fmaf(l, alpha, ps);
#pragma unroll
      for (int i = 0; i < 8; ++i) acc[i] *= alpha;
#pragma unroll
      for (int u = 0; u < U; ++u) {
#pragma unroll
        for (int j = 0; j < 4; ++j) {
          const unsigned w = Vr[g][u][j];
          acc[2 * j] = fmaf(p[u], __builtin_bit_cast(float, w << 16), acc[2 * j]);
          acc[2 * j + 1] = fmaf(p[u], __builtin_bit_cast(float, w & 0xffff0000u), acc[2 * j + 1]);
        }
      }
#pragma unroll
      for (int u = 0; u < U; ++u) Vr[g][u] = __builtin_amdgcn_raw_buffer_load_b128(rv, voff + u * 1024, so, AUX);
    }
  }
  float* rec = lds + (wave * 4 + r) * 132;
  *reinterpret_cast<f32x4*>(rec + 4 + 8 * c) = f32x4{acc[0], acc[1], acc[2], acc[3]};
  *reinterpret_cast<f32x4*>(rec + 8 + 8 * c) = f32x4{acc[4], acc[5], acc[6], acc[7]};
  if (c == 0) { rec[0] = m; rec[1] = l; }
  __syncthreads();
  if (tid < D) {
    float M = -INFINITY;
#pragma unroll
    for (int i = 0; i < NWAVE * 4; ++i) M = fmaxf(M, lds[i * 132]);
    float L = 0.f, o = 0.f;
#pragma unroll
    for (int i = 0; i < NWAVE * 4; ++i) {
      const float w = __builtin_amdgcn_exp2f(lds[i * 132] - M);
      L = fmaf(w, lds[i * 132 + 1], L);
      o = fmaf(w, lds[i * 132 + 4 + tid], o);
    }
    float* prec = ws + (size_t)bid * 132;
    prec[4 + tid] = o;
    if (tid == 0) { prec[0] = M; prec[1] = L; }
  }
}

__global__ void __launch_bounds__(128) attn_comb(const float* __restrict__ ws, __hip_bfloat16* __restrict__ out) {
  const int bh = blockIdx.x, tid = threadIdx.x;
  const float* p = ws + (size_t)bh * NSPLIT * 132;
  float M = -INFINITY;
#pragma unroll
  for (int i = 0; i < NSPLIT; ++i) M = fmaxf(M, p[i * 132]);
  float L = 0.f, o = 0.f;
#pragma unroll
  for (int i = 0; i < NSPLIT; ++i) {
    const float w = __builtin_amdgcn_exp2f(p[i * 132] - M);
    L = fmaf(w, p[i * 132 + 1], L);
    o = fmaf(w, p[i * 132 + 4 + tid], o);
  }
  out[bh * D + tid] = __float2bfloat16(o / L);
}

extern "C" void kernel_launch(void* const* d_in, const int* in_sizes, int n_in, void* d_out, int out_size, void* d_ws,
                              size_t ws_size, hipStream_t stream) {
  const uint16_t* q = (const uint16_t*)d_in[0];
  const uint16_t* k = (const uint16_t*)d_in[1];
  const uint16_t* v = (const uint16_t*)d_in[2];
  attn_part<<<BH * NSPLIT, NWAVE * 64, 0, stream>>>(q, k, v, (float*)d_ws);
  attn_comb<<<BH, 128, 0, stream>>>((const float*)d_ws, (__hip_bfloat16*)d_out);
}
```

```cpp
#include <hip/hip_runtime.h>
#include <hip/hip_bf16.h>
#include <stdint.h>

constexpr int BH = 128, SKV = 4096, D = 128;
#define NSPLIT 2
#define NWAVE 4
#define U 4
#define RING 2
#define AUX 2
#define XCDMAP 0
constexpr int KEYS_WG = SKV / NSPLIT;
constexpr int STEPS = KEYS_WG / (NWAVE * 4 * U);
static_assert(STEPS % RING == 0, "");

typedef unsigned u32x4 __attribute__((ext_vector_type(4)));
typedef __bf16 bf16x2 __attribute__((ext_vector_type(2)));
typedef float f32x4 __attribute__((ext_vector_type(4)));

__device__ __forceinline__ float dot2(unsigned a, unsigned b, float acc) {
  return __builtin_amdgcn_fdot2_f32_bf16(__builtin_bit_cast(bf16x2, a), __builtin_bit_cast(bf16x2, b), acc, false);
}
template <int CTRL> __device__ __forceinline__ float dpp(float x) {
  return __builtin_bit_cast(float, __builtin_amdgcn_mov_dpp(__builtin_bit_cast(int, x), CTRL, 0xf, 0xf, true));
}
__device__ __forceinline__ float row_allsum(float x) {
  x += dpp<0x128>(x); x += dpp<0x124>(x); x += dpp<0x122>(x); x += dpp<0x121>(x);
  return x;
}
#define SB() __builtin_amdgcn_sched_barrier(0)

__global__ void __launch_bounds__(NWAVE * 64) attn_part(const uint16_t* __restrict__ q, const uint16_t* __restrict__ k,
                                                        const uint16_t* __restrict__ v, float* __restrict__ ws) {
  __shared__ float lds[NWAVE * 4 * 132];
  int bid = blockIdx.x;
  if (XCDMAP) bid = (bid & 7) * (BH * NSPLIT / 8) + (bid >> 3);
  const int bh = bid / NSPLIT, sp = bid % NSPLIT;
  const int tid = threadIdx.x, lane = tid & 63, wave = __builtin_amdgcn_readfirstlane(tid >> 6);
  const int r = lane >> 4, c = lane & 15;
  constexpr float C2 = 0.088388347648318440f * 1.4426950408889634f;
  const size_t base = ((size_t)bh * SKV + (size_t)sp * KEYS_WG) * D;
  auto rk = __builtin_amdgcn_make_buffer_rsrc((void*)(k + base), 0, KEYS_WG * D * 2, 0x00020000);
  auto rv = __builtin_amdgcn_make_buffer_rsrc((void*)(v + base), 0, KEYS_WG * D * 2, 0x00020000);
  const int voff = lane * 16;
  const u32x4 qv = *reinterpret_cast<const u32x4*>(q + (size_t)bh * D + 8 * c);
  SB();
  u32x4 Kr[RING][U], Vr[RING][U];
#pragma unroll
  for (int g = 0; g < RING; ++g) {
    const int so = (g * NWAVE + wave) * U * 1024;
#pragma unroll
    for (int u = 0; u < U; ++u) Kr[g][u] = __builtin_amdgcn_raw_buffer_load_b128(rk, voff + u * 1024, so, AUX);
    SB();
#pragma unroll
    for (int u = 0; u < U; ++u) Vr[g][u] = __builtin_amdgcn_raw_buffer_load_b128(rv, voff + u * 1024, so, AUX);
    SB();
  }
  float m = -INFINITY, l = 0.f, acc[8];
#pragma unroll
  for (int i = 0; i < 8; ++i) acc[i] = 0.f;
  for (int s0 = 0; s0 < STEPS; s0 += RING) {
#pragma unroll
    for (int g = 0; g < RING; ++g) {
      const int so = ((s0 + g + RING) * NWAVE + wave) * U * 1024;
      float sc[U];
#pragma unroll
      for (int u = 0; u < U; ++u) {
        float d = dot2(Kr[g][u][0], qv[0], 0.f);
        d = dot2(Kr[g][u][1], qv[1], d); d = dot2(Kr[g][u][2], qv[2], d); d = dot2(Kr[g][u][3], qv[3], d);
        sc[u] = d;
        asm volatile("" : "+v"(sc[u]));
      }
      SB();
#pragma unroll
      for (int u = 0; u < U; ++u) Kr[g][u] = __builtin_amdgcn_raw_buffer_load_b128(rk, voff + u * 1024, so, AUX);
      SB();
#pragma unroll
      for (int u = 0; u < U; ++u) sc[u] = row_allsum(sc[u]) * C2;
      float mn = m;
#pragma unroll
      for (int u = 0; u < U; ++u) mn = fmaxf(mn, sc[u]);
      const float alpha = __builtin_amdgcn_exp2f(m - mn);
      m = mn;
      float p[U], ps = 0.f;
#pragma unroll
      for (int u = 0; u < U; ++u) { p[u] = __builtin_amdgcn_exp2f(sc[u] - mn); ps += p[u]; }
      l = fmaf(l, alpha, ps);
#pragma unroll
      for (int i = 0; i < 8; ++i) acc[i] *= alpha;
#pragma unroll
      for (int u = 0; u < U; ++u) {
#pragma unroll
        for (int j = 0; j < 4; ++j) {
          const unsigned w = Vr[g][u][j];
          acc[2 * j] = fmaf(p[u], __builtin_bit_cast(float, w << 16), acc[2 * j]);
          acc[2 * j + 1] = fmaf(p[u], __builtin_bit_cast(float, w & 0xffff0000u), acc[2 * j + 1]);
        }
      }
      asm volatile("" : "+v"(acc[0]), "+v"(acc[1]), "+v"(acc[2]), "+v"(acc[3]), "+v"(acc[4]), "+v"(acc[5]), "+v"(acc[6]), "+v"(acc[7]), "+v"(l));
      SB();
#pragma unroll
      for (int u = 0; u < U; ++u) Vr[g][u] = __builtin_amdgcn_raw_buffer_load_b128(rv, voff + u * 1024, so, AUX);
      SB();
    }
  }
  float* rec = lds + (wave * 4 + r) * 132;
  *reinterpret_cast<f32x4*>(rec + 4 + 8 * c) = f32x4{acc[0], acc[1], acc[2], acc[3]};
  *reinterpret_cast<f32x4*>(rec + 8 + 8 * c) = f32x4{acc[4], acc[5], acc[6], acc[7]};
  if (c == 0) { rec[0] = m; rec[1] = l; }
  __syncthreads();
  if (tid < D) {
    float M = -INFINITY;
#pragma unroll
    for (int i = 0; i < NWAVE * 4; ++i) M = fmaxf(M, lds[i * 132]);
    float L = 0.f, o = 0.f;
#pragma unroll
    for (int i = 0; i < NWAVE * 4; ++i) {
      const float w = __builtin_amdgcn_exp2f(lds[i * 132] - M);
      L = fmaf(w, lds[i * 132 + 1], L);
      o = fmaf(w, lds[i * 132 + 4 + tid], o);
    }
    float* prec = ws + (size_t)bid * 132;
    prec[4 + tid] = o;
    if (tid == 0) { prec[0] = M; prec[1] = L; }
  }
}

__global__ void __launch_bounds__(128) attn_comb(const float* __restrict__ ws, __hip_bfloat16* __restrict__ out) {
  const int bh = blockIdx.x, tid = threadIdx.x;
  const float* p = ws + (size_t)bh * NSPLIT * 132;
  float M = -INFINITY;
#pragma unroll
  for (int i = 0; i < NSPLIT; ++i) M = fmaxf(M, p[i * 132]);
  float L = 0.f, o = 0.f;
#pragma unroll
  for (int i = 0; i < NSPLIT; ++i) {
    const float w = __builtin_amdgcn_exp2f(p[i * 132] - M);
    L = fmaf(w, p[i * 132 + 1], L);
    o = fmaf(w, p[i * 132 + 4 + tid], o);
  }
  out[bh * D + tid] = __float2bfloat16(o / L);
}

extern "C" void kernel_launch(void* const* d_in, const int* in_sizes, int n_in, void* d_out, int out_size, void* d_ws,
                              size_t ws_size, hipStream_t stream) {
  const uint16_t* q = (const uint16_t*)d_in[0];
  const uint16_t* k = (const uint16_t*)d_in[1];
  const uint16_t* v = (const uint16_t*)d_in[2];
  attn_part<<<BH * NSPLIT, NWAVE * 64, 0, stream>>>(q, k, v, (float*)d_ws);
  attn_comb<<<BH, 128, 0, stream>>>((const float*)d_ws, (__hip_bfloat16*)d_out);
}
```

```cpp
#include <hip/hip_runtime.h>
#include <hip/hip_bf16.h>
#include <stdint.h>

constexpr int BH = 128, SKV = 4096, D = 128;
#define NSPLIT 2
#define NWAVE 4
#define U 4
#define RING 2
#define AUXK 2
#define AUXV 0
#define XCDMAP 0
constexpr int KEYS_WG = SKV / NSPLIT;
constexpr int STEPS = KEYS_WG / (NWAVE * 4 * U);
static_assert(STEPS % RING == 0, "");

typedef unsigned u32x4 __attribute__((ext_vector_type(4)));
typedef __bf16 bf16x2 __attribute__((ext_vector_type(2)));
typedef float f32x4 __attribute__((ext_vector_type(4)));

__device__ __forceinline__ float dot2(unsigned a, unsigned b, float acc) {
  return __builtin_amdgcn_fdot2_f32_bf16(__builtin_bit_cast(bf16x2, a), __builtin_bit_cast(bf16x2, b), acc, false);
}
template <int CTRL> __device__ __forceinline__ float dpp(float x) {
  return __builtin_bit_cast(float, __builtin_amdgcn_mov_dpp(__builtin_bit_cast(int, x), CTRL, 0xf, 0xf, true));
}
__device__ __forceinline__ float row_allsum(float x) {
  x += dpp<0x128>(x); x += dpp<0x124>(x); x += dpp<0x122>(x); x += dpp<0x121>(x);
  return x;
}
#define SB() __builtin_amdgcn_sched_barrier(0)

__global__ void __launch_bounds__(NWAVE * 64) attn_part(const uint16_t* __restrict__ q, const uint16_t* __restrict__ k,
                                                        const uint16_t* __restrict__ v, float* __restrict__ ws) {
  __shared__ float lds[NWAVE * 4 * 132];
  int bid = blockIdx.x;
  if (XCDMAP) bid = (bid & 7) * (BH * NSPLIT / 8) + (bid >> 3);
  const int bh = bid / NSPLIT, sp = bid % NSPLIT;
  const int tid = threadIdx.x, lane = tid & 63, wave = __builtin_amdgcn_readfirstlane(tid >> 6);
  const int r = lane >> 4, c = lane & 15;
  constexpr float C2 = 0.088388347648318440f * 1.4426950408889634f;
  const size_t base = ((size_t)bh * SKV + (size_t)sp * KEYS_WG) * D;
  auto rk = __builtin_amdgcn_make_buffer_rsrc((void*)(k + base), 0, KEYS_WG * D * 2, 0x00020000);
  auto rv = __builtin_amdgcn_make_buffer_rsrc((void*)(v + base), 0, KEYS_WG * D * 2, 0x00020000);
  const int voff = lane * 16;
  const u32x4 qv = *reinterpret_cast<const u32x4*>(q + (size_t)bh * D + 8 * c);
  SB();
  u32x4 Kr[RING][U], Vr[RING][U];
#pragma unroll
  for (int g = 0; g < RING; ++g) {
    const int so = (g * NWAVE + wave) * U * 1024;
#pragma unroll
    for (int u = 0; u < U; ++u) Kr[g][u] = __builtin_amdgcn_raw_buffer_load_b128(rk, voff + u * 1024, so, AUXK);
    SB();
#pragma unroll
    for (int u = 0; u < U; ++u) Vr[g][u] = __builtin_amdgcn_raw_buffer_load_b128(rv, voff + u * 1024, so, AUXV);
    SB();
  }
  float m = -INFINITY, l = 0.f, acc[8];
#pragma unroll
  for (int i = 0; i < 8; ++i) acc[i] = 0.f;
  for (int s0 = 0; s0 < STEPS; s0 += RING) {
#pragma unroll
    for (int g = 0; g < RING; ++g) {
      const int so = ((s0 + g + RING) * NWAVE + wave) * U * 1024;
      float sc[U];
#pragma unroll
      for (int u = 0; u < U; ++u) {
        float d = dot2(Kr[g][u][0], qv[0], 0.f);
        d = dot2(Kr[g][u][1], qv[1], d); d = dot2(Kr[g][u][2], qv[2], d); d = dot2(Kr[g][u][3], qv[3], d);
        sc[u] = d;
        asm volatile("" : "+v"(sc[u]));
      }
      SB();
#pragma unroll
      for (int u = 0; u < U; ++u) Kr[g][u] = __builtin_amdgcn_raw_buffer_load_b128(rk, voff + u * 1024, so, AUXK);
      SB();
#pragma unroll
      for (int u = 0; u < U; ++u) sc[u] = row_allsum(sc[u]) * C2;
      float mn = m;
#pragma unroll
      for (int u = 0; u < U; ++u) mn = fmaxf(mn, sc[u]);
      const float alpha = __builtin_amdgcn_exp2f(m - mn);
      m = mn;
      float p[U], ps = 0.f;
#pragma unroll
      for (int u = 0; u < U; ++u) { p[u] = __builtin_amdgcn_exp2f(sc[u] - mn); ps += p[u]; }
      l = fmaf(l, alpha, ps);
#pragma unroll
      for (int i = 0; i < 8; ++i) acc[i] *= alpha;
#pragma unroll
      for (int u = 0; u < U; ++u) {
#pragma unroll
        for (int j = 0; j < 4; ++j) {
          const unsigned w = Vr[g][u][j];
          acc[2 * j] = fmaf(p[u], __builtin_bit_cast(float, w << 16), acc[2 * j]);
          acc[2 * j + 1] = fmaf(p[u], __builtin_bit_cast(float, w & 0xffff0000u), acc[2 * j + 1]);
        }
      }
      asm volatile("" : "+v"(acc[0]), "+v"(acc[1]), "+v"(acc[2]), "+v"(acc[3]), "+v"(acc[4]), "+v"(acc[5]), "+v"(acc[6]), "+v"(acc[7]), "+v"(l));
      SB();
#pragma unroll
      for (int u = 0; u < U; ++u) Vr[g][u] = __builtin_amdgcn_raw_buffer_load_b128(rv, voff + u * 1024, so, AUXV);
      SB();
    }
  }
  float* rec = lds + (wave * 4 + r) * 132;
  *reinterpret_cast<f32x4*>(rec + 4 + 8 * c) = f32x4{acc[0], acc[1], acc[2], acc[3]};
  *reinterpret_cast<f32x4*>(rec + 8 + 8 * c) = f32x4{acc[4], acc[5], acc[6], acc[7]};
  if (c == 0) { rec[0] = m; rec[1] = l; }
  __syncthreads();
  if (tid < D) {
    float M = -INFINITY;
#pragma unroll
    for (int i = 0; i < NWAVE * 4; ++i) M = fmaxf(M, lds[i * 132]);
    float L = 0.f, o = 0.f;
#pragma unroll
    for (int i = 0; i < NWAVE * 4; ++i) {
      const float w = __builtin_amdgcn_exp2f(lds[i * 132] - M);
      L = fmaf(w, lds[i * 132 + 1], L);
      o = fmaf(w, lds[i * 132 + 4 + tid], o);
    }
    float* prec = ws + (size_t)bid * 132;
    prec[4 + tid] = o;
    if (tid == 0) { prec[0] = M; prec[1] = L; }
  }
}

__global__ void __launch_bounds__(128) attn_comb(const float* __restrict__ ws, __hip_bfloat16* __restrict__ out) {
  const int bh = blockIdx.x, tid = threadIdx.x;
  const float* p = ws + (size_t)bh * NSPLIT * 132;
  float M = -INFINITY;
#pragma unroll
  for (int i = 0; i < NSPLIT; ++i) M = fmaxf(M, p[i * 132]);
  float L = 0.f, o = 0.f;
#pragma unroll
  for (int i = 0; i < NSPLIT; ++i) {
    const float w = __builtin_amdgcn_exp2f(p[i * 132] - M);
    L = fmaf(w, p[i * 132 + 1], L);
    o = fmaf(w, p[i * 132 + 4 + tid], o);
  }
  out[bh * D + tid] = __float2bfloat16(o / L);
}

extern "C" void kernel_launch(void* const* d_in, const int* in_sizes, int n_in, void* d_out, int out_size, void* d_ws,
                              size_t ws_size, hipStream_t stream) {
  const uint16_t* q = (const uint16_t*)d_in[0];
  const uint16_t* k = (const uint16_t*)d_in[1];
  const uint16_t* v = (const uint16_t*)d_in[2];
  attn_part<<<BH * NSPLIT, NWAVE * 64, 0, stream>>>(q, k, v, (float*)d_ws);
  attn_comb<<<BH, 128, 0, stream>>>((const float*)d_ws, (__hip_bfloat16*)d_out);
}
```

```cpp
#include <hip/hip_runtime.h>
#include <hip/hip_bf16.h>
#include <stdint.h>

constexpr int BH = 128, SKV = 4096, D = 128;
#define NWAVE 8
#define U 4
#define RING 1
#define AUX 2
#define XCDMAP 1
constexpr int NSPLIT = 2;
constexpr int NWG = BH * NSPLIT;
constexpr int KEYS_WG = SKV / NSPLIT;
constexpr int STEPS = KEYS_WG / (NWAVE * 4 * U);
static_assert(STEPS % RING == 0, "");
constexpr unsigned TOK_ENTRY = 0x454e5452u, TOK_READY = 0x52454459u, CLEARED = 0xffffffffu;
constexpr size_t WS_MBOX = (size_t)NWG * 1024;

typedef unsigned u32x4 __attribute__((ext_vector_type(4)));
typedef __bf16 bf16x2 __attribute__((ext_vector_type(2)));
typedef float f32x4 __attribute__((ext_vector_type(4)));
typedef float f32x2 __attribute__((ext_vector_type(2)));
#define GAS __attribute__((address_space(1)))
typedef GAS unsigned gu32;
typedef GAS unsigned long long gu64;
#define RLX_AGENT __ATOMIC_RELAXED, __HIP_MEMORY_SCOPE_AGENT

__device__ __forceinline__ float dot2(unsigned a, unsigned b, float acc) {
  return __builtin_amdgcn_fdot2_f32_bf16(__builtin_bit_cast(bf16x2, a), __builtin_bit_cast(bf16x2, b), acc, false);
}
template <int CTRL> __device__ __forceinline__ float dpp(float x) {
  return __builtin_bit_cast(float, __builtin_amdgcn_mov_dpp(__builtin_bit_cast(int, x), CTRL, 0xf, 0xf, true));
}
__device__ __forceinline__ float row_allsum(float x) {
  x += dpp<0x128>(x); x += dpp<0x124>(x); x += dpp<0x122>(x); x += dpp<0x121>(x);
  return x;
}
__device__ __forceinline__ float xrow_max(float x) {
  auto s = __builtin_amdgcn_permlane16_swap(__float_as_uint(x), __float_as_uint(x), false, false);
  x = fmaxf(__uint_as_float(s[0]), __uint_as_float(s[1]));
  auto t = __builtin_amdgcn_permlane32_swap(__float_as_uint(x), __float_as_uint(x), false, false);
  return fmaxf(__uint_as_float(t[0]), __uint_as_float(t[1]));
}
__device__ __forceinline__ float xrow_sum(float x) {
  auto s = __builtin_amdgcn_permlane16_swap(__float_as_uint(x), __float_as_uint(x), false, false);
  x = __uint_as_float(s[0]) + __uint_as_float(s[1]);
  auto t = __builtin_amdgcn_permlane32_swap(__float_as_uint(x), __float_as_uint(x), false, false);
  return __uint_as_float(t[0]) + __uint_as_float(t[1]);
}
#define SB() __builtin_amdgcn_sched_barrier(0)
#define DRAIN() asm volatile("s_waitcnt vmcnt(0)" ::: "memory")

__global__ void __launch_bounds__((NWAVE + 1) * 64) attn_decode(const uint16_t* __restrict__ q, const uint16_t* __restrict__ k,
                                                                const uint16_t* __restrict__ v, __hip_bfloat16* __restrict__ out,
                                                                unsigned char* __restrict__ ws) {
  __shared__ __attribute__((aligned(16))) float lds[NWAVE * 132];
  int bid = blockIdx.x;
  if (XCDMAP) bid = (bid & 7) * (NWG / 8) + (bid >> 3);
  const int bh = bid >> 1, sp = bid & 1;
  const int tid = threadIdx.x, lane = tid & 63, wave = __builtin_amdgcn_readfirstlane(tid >> 6);
  if (wave < NWAVE) {
    const int r = lane >> 4, c = lane & 15;
    constexpr float C2 = 0.088388347648318440f * 1.4426950408889634f;
    const size_t base = ((size_t)bh * SKV + (size_t)sp * KEYS_WG) * D;
    auto rk = __builtin_amdgcn_make_buffer_rsrc((void*)(k + base), 0, KEYS_WG * D * 2, 0x00020000);
    auto rv = __builtin_amdgcn_make_buffer_rsrc((void*)(v + base), 0, KEYS_WG * D * 2, 0x00020000);
    const int voff = lane * 16;
    const u32x4 qv = *reinterpret_cast<const u32x4*>(q + (size_t)bh * D + 8 * c);
    SB();
    u32x4 Kr[RING][U], Vr[RING][U];
#pragma unroll
    for (int g = 0; g < RING; ++g) {
      const int so = (g * NWAVE + wave) * U * 1024;
#pragma unroll
      for (int u = 0; u < U; ++u) Kr[g][u] = __builtin_amdgcn_raw_buffer_load_b128(rk, voff + u * 1024, so, AUX);
      SB();
#pragma unroll
      for (int u = 0; u < U; ++u) Vr[g][u] = __builtin_amdgcn_raw_buffer_load_b128(rv, voff + u * 1024, so, AUX);
      SB();
    }
    float m = -INFINITY, l = 0.f, acc[8];
#pragma unroll
    for (int i = 0; i < 8; ++i) acc[i] = 0.f;
    for (int s0 = 0; s0 < STEPS; s0 += RING) {
#pragma unroll
      for (int g = 0; g < RING; ++g) {
        const int so = ((s0 + g + RING) * NWAVE + wave) * U * 1024;
        float sc[U];
#pragma unroll
        for (int u = 0; u < U; ++u) {
          float d = dot2(Kr[g][u][0], qv[0], 0.f);
          d = dot2(Kr[g][u][1], qv[1], d); d = dot2(Kr[g][u][2], qv[2], d); d = dot2(Kr[g][u][3], qv[3], d);
          sc[u] = d;
          asm volatile("" : "+v"(sc[u]));
        }
        SB();
#pragma unroll
        for (int u = 0; u < U; ++u) Kr[g][u] = __builtin_amdgcn_raw_buffer_load_b128(rk, voff + u * 1024, so, AUX);
        SB();
#pragma unroll
        for (int u = 0; u < U; ++u) sc[u] = row_allsum(sc[u]) * C2;
        float mn = m;
#pragma unroll
        for (int u = 0; u < U; ++u) mn = fmaxf(mn, sc[u]);
        const float alpha = __builtin_amdgcn_exp2f(m - mn);
        m = mn;
        float p[U], ps = 0.f;
#pragma unroll
        for (int u = 0; u < U; ++u) { p[u] = __builtin_amdgcn_exp2f(sc[u] - mn); ps += p[u]; }
        l = fmaf(l, alpha, ps);
#pragma unroll
        for (int i = 0; i < 8; ++i) acc[i] *= alpha;
#pragma unroll
        for (int u = 0; u < U; ++u) {
#pragma unroll
          for (int j = 0; j < 4; ++j) {
            const unsigned w = Vr[g][u][j];
            acc[2 * j] = fmaf(p[u], __builtin_bit_cast(float, w << 16), acc[2 * j]);
            acc[2 * j + 1] = fmaf(p[u], __builtin_bit_cast(float, w & 0xffff0000u), acc[2 * j + 1]);
          }
        }
        asm volatile("" : "+v"(acc[0]), "+v"(acc[1]), "+v"(acc[2]), "+v"(acc[3]), "+v"(acc[4]), "+v"(acc[5]), "+v"(acc[6]), "+v"(acc[7]), "+v"(l));
        SB();
#pragma unroll
        for (int u = 0; u < U; ++u) Vr[g][u] = __builtin_amdgcn_raw_buffer_load_b128(rv, voff + u * 1024, so, AUX);
        SB();
      }
    }
    const float mw = xrow_max(m);
    const float sc = __builtin_amdgcn_exp2f(m - mw);
    l = xrow_sum(l * sc);
#pragma unroll
    for (int i = 0; i < 8; ++i) acc[i] = xrow_sum(acc[i] * sc);
    float* rec = lds + wave * 132;
    if (r == 0) {
      *reinterpret_cast<f32x4*>(rec + 4 + 8 * c) = f32x4{acc[0], acc[1], acc[2], acc[3]};
      *reinterpret_cast<f32x4*>(rec + 8 + 8 * c) = f32x4{acc[4], acc[5], acc[6], acc[7]};
      if (c == 0) *reinterpret_cast<f32x2*>(rec) = f32x2{mw, l};
    }
    __syncthreads();
    return;
  }
  gu64* inbox_me = (gu64*)(ws + (size_t)bid * 1024) + lane;
  gu64* inbox_pt = (gu64*)(ws + (size_t)(bid ^ 1) * 1024) + lane;
  gu32* mbox_me = (gu32*)(ws + WS_MBOX + (size_t)bid * 128);
  gu32* mbox_pt = (gu32*)(ws + WS_MBOX + (size_t)(bid ^ 1) * 128);
  const unsigned long long t0 = __builtin_amdgcn_s_memrealtime();
  constexpr unsigned long long TMO = 20000000ull;
  __hip_atomic_store(inbox_me, ~0ull, RLX_AGENT);
  __hip_atomic_store(inbox_me + 64, ~0ull, RLX_AGENT);
  if (lane == 0) __hip_atomic_store(mbox_me, TOK_ENTRY, RLX_AGENT);
  DRAIN();
  if (lane == 0) __hip_atomic_store(mbox_pt, TOK_READY, RLX_AGENT);
  DRAIN();
  for (bool got = false;;) {
    const unsigned own = __builtin_amdgcn_readfirstlane(__hip_atomic_load(mbox_me, RLX_AGENT));
    const unsigned par = __builtin_amdgcn_readfirstlane(__hip_atomic_load(mbox_pt, RLX_AGENT));
    if (par == TOK_ENTRY) {
      if (lane == 0) __hip_atomic_store(mbox_pt, TOK_READY, RLX_AGENT);
      DRAIN();
    }
    if (got) break;
    if (own == TOK_READY) { got = true; continue; }
    if (__builtin_amdgcn_s_memrealtime() - t0 > TMO) break;
    __builtin_amdgcn_s_sleep(16);
  }
  __syncthreads();
  unsigned long long x0 = __hip_atomic_load(inbox_me, RLX_AGENT), x1 = __hip_atomic_load(inbox_me + 64, RLX_AGENT);
  float M = -INFINITY;
  f32x2 ml[NWAVE];
#pragma unroll
  for (int w = 0; w < NWAVE; ++w) { ml[w] = *reinterpret_cast<const f32x2*>(lds + w * 132); M = fmaxf(M, ml[w][0]); }
  float L = 0.f, o0 = 0.f, o1 = 0.f;
#pragma unroll
  for (int w = 0; w < NWAVE; ++w) {
    const float wt = __builtin_amdgcn_exp2f(ml[w][0] - M);
    L = fmaf(wt, ml[w][1], L);
    o0 = fmaf(wt, lds[w * 132 + 4 + lane], o0);
    o1 = fmaf(wt, lds[w * 132 + 68 + lane], o1);
  }
  const float inv = 1.0f / L;
  o0 *= inv; o1 *= inv;
  const float lse = M + __builtin_amdgcn_logf(L);
  unsigned tag = __float_as_uint(lse);
  if (tag == CLEARED) tag = 0x7fc00000u;
  __hip_atomic_store(inbox_pt, ((unsigned long long)tag << 32) | __float_as_uint(o0), RLX_AGENT);
  __hip_atomic_store(inbox_pt + 64, ((unsigned long long)tag << 32) | __float_as_uint(o1), RLX_AGENT);
  bool have = __all((unsigned)(x0 >> 32) != CLEARED && (unsigned)(x1 >> 32) != CLEARED);
  if (!have) {
    DRAIN();
    x0 = __hip_atomic_load(inbox_me, RLX_AGENT); x1 = __hip_atomic_load(inbox_me + 64, RLX_AGENT);
    have = __all((unsigned)(x0 >> 32) != CLEARED && (unsigned)(x1 >> 32) != CLEARED);
  }
  if (have) {
    const float lse_me = __uint_as_float(tag), lse_pt = __uint_as_float((unsigned)(x0 >> 32));
    const float lseA = sp == 0 ? lse_me : lse_pt, lseB = sp == 0 ? lse_pt : lse_me;
    const float p0 = __uint_as_float((unsigned)x0), p1 = __uint_as_float((unsigned)x1);
    const float a0 = sp == 0 ? o0 : p0, b0 = sp == 0 ? p0 : o0, a1 = sp == 0 ? o1 : p1, b1 = sp == 0 ? p1 : o1;
    const float MM = fmaxf(lseA, lseB);
    const float wA = __builtin_amdgcn_exp2f(lseA - MM), wB = __builtin_amdgcn_exp2f(lseB - MM);
    const float iw = 1.0f / (wA + wB);
    out[bh * D + lane] = __float2bfloat16(fmaf(wA, a0, wB * b0) * iw);
    out[bh * D + 64 + lane] = __float2bfloat16(fmaf(wA, a1, wB * b1) * iw);
  }
}

extern "C" void kernel_launch(void* const* d_in, const int* in_sizes, int n_in, void* d_out, int out_size, void* d_ws,
                              size_t ws_size, hipStream_t stream) {
  attn_decode<<<NWG, (NWAVE + 1) * 64, 0, stream>>>((const uint16_t*)d_in[0], (const uint16_t*)d_in[1], (const uint16_t*)d_in[2],
                                                    (__hip_bfloat16*)d_out, (unsigned char*)d_ws);
}
```

```cpp
#include <hip/hip_runtime.h>
#include <hip/hip_bf16.h>
#include <stdint.h>

constexpr int BH = 128, SKV = 4096, D = 128;
#define NWAVE 8
#define U 4
#define RING 1
#define AUX 2
#define XCDMAP 0
constexpr int NSPLIT = 2;
constexpr int NWG = BH * NSPLIT;
constexpr int KEYS_WG = SKV / NSPLIT;
constexpr int STEPS = KEYS_WG / (NWAVE * 4 * U);
static_assert(STEPS % RING == 0, "");
constexpr unsigned TOK_ENTRY = 0x454e5452u, TOK_READY = 0x52454459u, CLEARED = 0xffffffffu;
constexpr size_t WS_MBOX = (size_t)NWG * 1024;

typedef unsigned u32x4 __attribute__((ext_vector_type(4)));
typedef __bf16 bf16x2 __attribute__((ext_vector_type(2)));
typedef float f32x4 __attribute__((ext_vector_type(4)));
typedef float f32x2 __attribute__((ext_vector_type(2)));
#define GAS __attribute__((address_space(1)))
typedef GAS unsigned gu32;
typedef GAS unsigned long long gu64;
#define RLX_AGENT __ATOMIC_RELAXED, __HIP_MEMORY_SCOPE_AGENT

__device__ __forceinline__ float dot2(unsigned a, unsigned b, float acc) {
  return __builtin_amdgcn_fdot2_f32_bf16(__builtin_bit_cast(bf16x2, a), __builtin_bit_cast(bf16x2, b), acc, false);
}
template <int CTRL> __device__ __forceinline__ float dpp(float x) {
  return __builtin_bit_cast(float, __builtin_amdgcn_mov_dpp(__builtin_bit_cast(int, x), CTRL, 0xf, 0xf, true));
}
__device__ __forceinline__ float row_allsum(float x) {
  x += dpp<0x128>(x); x += dpp<0x124>(x); x += dpp<0x122>(x); x += dpp<0x121>(x);
  return x;
}
__device__ __forceinline__ float xrow_max(float x) {
  auto s = __builtin_amdgcn_permlane16_swap(__float_as_uint(x), __float_as_uint(x), false, false);
  x = fmaxf(__uint_as_float(s[0]), __uint_as_float(s[1]));
  auto t = __builtin_amdgcn_permlane32_swap(__float_as_uint(x), __float_as_uint(x), false, false);
  return fmaxf(__uint_as_float(t[0]), __uint_as_float(t[1]));
}
__device__ __forceinline__ float xrow_sum(float x) {
  auto s = __builtin_amdgcn_permlane16_swap(__float_as_uint(x), __float_as_uint(x), false, false);
  x = __uint_as_float(s[0]) + __uint_as_float(s[1]);
  auto t = __builtin_amdgcn_permlane32_swap(__float_as_uint(x), __float_as_uint(x), false, false);
  return __uint_as_float(t[0]) + __uint_as_float(t[1]);
}
#define SB() __builtin_amdgcn_sched_barrier(0)
#define DRAIN() asm volatile("s_waitcnt vmcnt(0)" ::: "memory")

template <int AUXK, int AUXV>
__device__ __forceinline__ void stream_part(const uint16_t* __restrict__ q, const uint16_t* __restrict__ k, const uint16_t* __restrict__ v,
                                            float* lds, int bh, int sp, int lane, int wave) {
    const int r = lane >> 4, c = lane & 15;
    constexpr float C2 = 0.088388347648318440f * 1.4426950408889634f;
    const size_t base = ((size_t)bh * SKV + (size_t)sp * KEYS_WG) * D;
    auto rk = __builtin_amdgcn_make_buffer_rsrc((void*)(k + base), 0, KEYS_WG * D * 2, 0x00020000);
    auto rv = __builtin_amdgcn_make_buffer_rsrc((void*)(v + base), 0, KEYS_WG * D * 2, 0x00020000);
    const int voff = lane * 16;
    const u32x4 qv = *reinterpret_cast<const u32x4*>(q + (size_t)bh * D + 8 * c);
    SB();
    u32x4 Kr[RING][U], Vr[RING][U];
#pragma unroll
    for (int g = 0; g < RING; ++g) {
      const int so = (g * NWAVE + wave) * U * 1024;
#pragma unroll
      for (int u = 0; u < U; ++u) Kr[g][u] = __builtin_amdgcn_raw_buffer_load_b128(rk, voff + u * 1024, so, AUXK);
      SB();
#pragma unroll
      for (int u = 0; u < U; ++u) Vr[g][u] = __builtin_amdgcn_raw_buffer_load_b128(rv, voff + u * 1024, so, AUXV);
      SB();
    }
    float m = -INFINITY, l = 0.f, acc[8];
#pragma unroll
    for (int i = 0; i < 8; ++i) acc[i] = 0.f;
    for (int s0 = 0; s0 < STEPS; s0 += RING) {
#pragma unroll
      for (int g = 0; g < RING; ++g) {
        const int so = ((s0 + g + RING) * NWAVE + wave) * U * 1024;
        float sc[U];
#pragma unroll
        for (int u = 0; u < U; ++u) {
          float d = dot2(Kr[g][u][0], qv[0], 0.f);
          d = dot2(Kr[g][u][1], qv[1], d); d = dot2(Kr[g][u][2], qv[2], d); d = dot2(Kr[g][u][3], qv[3], d);
          sc[u] = d;
          asm volatile("" : "+v"(sc[u]));
        }
        SB();
#pragma unroll
        for (int u = 0; u < U; ++u) Kr[g][u] = __builtin_amdgcn_raw_buffer_load_b128(rk, voff + u * 1024, so, AUXK);
        SB();
#pragma unroll
        for (int u = 0; u < U; ++u) sc[u] = row_allsum(sc[u]) * C2;
        float mn = m;
#pragma unroll
        for (int u = 0; u < U; ++u) mn = fmaxf(mn, sc[u]);
        const float alpha = __builtin_amdgcn_exp2f(m - mn);
        m = mn;
        float p[U], ps = 0.f;
#pragma unroll
        for (int u = 0; u < U; ++u) { p[u] = __builtin_amdgcn_exp2f(sc[u] - mn); ps += p[u]; }
        l = fmaf(l, alpha, ps);
#pragma unroll
        for (int i = 0; i < 8; ++i) acc[i] *= alpha;
#pragma unroll
        for (int u = 0; u < U; ++u) {
#pragma unroll
          for (int j = 0; j < 4; ++j) {
            const unsigned w = Vr[g][u][j];
            acc[2 * j] = fmaf(p[u], __builtin_bit_cast(float, w << 16), acc[2 * j]);
            acc[2 * j + 1] = fmaf(p[u], __builtin_bit_cast(float, w & 0xffff0000u), acc[2 * j + 1]);
          }
        }
        asm volatile("" : "+v"(acc[0]), "+v"(acc[1]), "+v"(acc[2]), "+v"(acc[3]), "+v"(acc[4]), "+v"(acc[5]), "+v"(acc[6]), "+v"(acc[7]), "+v"(l));
        SB();
#pragma unroll
        for (int u = 0; u < U; ++u) Vr[g][u] = __builtin_amdgcn_raw_buffer_load_b128(rv, voff + u * 1024, so, AUXV);
        SB();
      }
    }
    const float mw = xrow_max(m);
    const float sc = __builtin_amdgcn_exp2f(m - mw);
    l = xrow_sum(l * sc);
#pragma unroll
    for (int i = 0; i < 8; ++i) acc[i] = xrow_sum(acc[i] * sc);
    float* rec = lds + wave * 132;
    if (r == 0) {
      *reinterpret_cast<f32x4*>(rec + 4 + 8 * c) = f32x4{acc[0], acc[1], acc[2], acc[3]};
      *reinterpret_cast<f32x4*>(rec + 8 + 8 * c) = f32x4{acc[4], acc[5], acc[6], acc[7]};
      if (c == 0) *reinterpret_cast<f32x2*>(rec) = f32x2{mw, l};
    }
}

__global__ void __launch_bounds__((NWAVE + 1) * 64) attn_decode(const uint16_t* __restrict__ q, const uint16_t* __restrict__ k,
                                                                const uint16_t* __restrict__ v, __hip_bfloat16* __restrict__ out,
                                                                unsigned char* __restrict__ ws) {
  __shared__ __attribute__((aligned(16))) float lds[NWAVE * 132];
  int bid = blockIdx.x;
  if (XCDMAP) bid = (bid & 7) * (NWG / 8) + (bid >> 3);
  const int bh = bid >> 1, sp = bid & 1;
  const int tid = threadIdx.x, lane = tid & 63, wave = __builtin_amdgcn_readfirstlane(tid >> 6);
  if (wave < NWAVE) {
    const bool kd = (bh<64), vd = (false);
    if (kd && vd) stream_part<0, 0>(q, k, v, lds, bh, sp, lane, wave);
    else if (kd) stream_part<0, 2>(q, k, v, lds, bh, sp, lane, wave);
    else if (vd) stream_part<2, 0>(q, k, v, lds, bh, sp, lane, wave);
    else stream_part<2, 2>(q, k, v, lds, bh, sp, lane, wave);
    __syncthreads();
    return;
  }
  gu64* inbox_me = (gu64*)(ws + (size_t)bid * 1024) + lane;
  gu64* inbox_pt = (gu64*)(ws + (size_t)(bid ^ 1) * 1024) + lane;
  gu32* mbox_me = (gu32*)(ws + WS_MBOX + (size_t)bid * 128);
  gu32* mbox_pt = (gu32*)(ws + WS_MBOX + (size_t)(bid ^ 1) * 128);
  const unsigned long long t0 = __builtin_amdgcn_s_memrealtime();
  constexpr unsigned long long TMO = 20000000ull;
  __hip_atomic_store(inbox_me, ~0ull, RLX_AGENT);
  __hip_atomic_store(inbox_me + 64, ~0ull, RLX_AGENT);
  if (lane == 0) __hip_atomic_store(mbox_me, TOK_ENTRY, RLX_AGENT);
  DRAIN();
  if (lane == 0) __hip_atomic_store(mbox_pt, TOK_READY, RLX_AGENT);
  DRAIN();
  for (bool got = false;;) {
    const unsigned own = __builtin_amdgcn_readfirstlane(__hip_atomic_load(mbox_me, RLX_AGENT));
    const unsigned par = __builtin_amdgcn_readfirstlane(__hip_atomic_load(mbox_pt, RLX_AGENT));
    if (par == TOK_ENTRY) {
      if (lane == 0) __hip_atomic_store(mbox_pt, TOK_READY, RLX_AGENT);
      DRAIN();
    }
    if (got) break;
    if (own == TOK_READY) { got = true; continue; }
    if (__builtin_amdgcn_s_memrealtime() - t0 > TMO) break;
    __builtin_amdgcn_s_sleep(16);
  }
  __syncthreads();
  unsigned long long x0 = __hip_atomic_load(inbox_me, RLX_AGENT), x1 = __hip_atomic_load(inbox_me + 64, RLX_AGENT);
  float M = -INFINITY;
  f32x2 ml[NWAVE];
#pragma unroll
  for (int w = 0; w < NWAVE; ++w) { ml[w] = *reinterpret_cast<const f32x2*>(lds + w * 132); M = fmaxf(M, ml[w][0]); }
  float L = 0.f, o0 = 0.f, o1 = 0.f;
#pragma unroll
  for (int w = 0; w < NWAVE; ++w) {
    const float wt = __builtin_amdgcn_exp2f(ml[w][0] - M);
    L = fmaf(wt, ml[w][1], L);
    o0 = fmaf(wt, lds[w * 132 + 4 + lane], o0);
    o1 = fmaf(wt, lds[w * 132 + 68 + lane], o1);
  }
  const float inv = 1.0f / L;
  o0 *= inv; o1 *= inv;
  const float lse = M + __builtin_amdgcn_logf(L);
  unsigned tag = __float_as_uint(lse);
  if (tag == CLEARED) tag = 0x7fc00000u;
  __hip_atomic_store(inbox_pt, ((unsigned long long)tag << 32) | __float_as_uint(o0), RLX_AGENT);
  __hip_atomic_store(inbox_pt + 64, ((unsigned long long)tag << 32) | __float_as_uint(o1), RLX_AGENT);
  bool have = __all((unsigned)(x0 >> 32) != CLEARED && (unsigned)(x1 >> 32) != CLEARED);
  if (!have) {
    DRAIN();
    x0 = __hip_atomic_load(inbox_me, RLX_AGENT); x1 = __hip_atomic_load(inbox_me + 64, RLX_AGENT);
    have = __all((unsigned)(x0 >> 32) != CLEARED && (unsigned)(x1 >> 32) != CLEARED);
  }
  if (have) {
    const float lse_me = __uint_as_float(tag), lse_pt = __uint_as_float((unsigned)(x0 >> 32));
    const float lseA = sp == 0 ? lse_me : lse_pt, lseB = sp == 0 ? lse_pt : lse_me;
    const float p0 = __uint_as_float((unsigned)x0), p1 = __uint_as_float((unsigned)x1);
    const float a0 = sp == 0 ? o0 : p0, b0 = sp == 0 ? p0 : o0, a1 = sp == 0 ? o1 : p1, b1 = sp == 0 ? p1 : o1;
    const float MM = fmaxf(lseA, lseB);
    const float wA = __builtin_amdgcn_exp2f(lseA - MM), wB = __builtin_amdgcn_exp2f(lseB - MM);
    const float iw = 1.0f / (wA + wB);
    out[bh * D + lane] = __float2bfloat16(fmaf(wA, a0, wB * b0) * iw);
    out[bh * D + 64 + lane] = __float2bfloat16(fmaf(wA, a1, wB * b1) * iw);
  }
}

extern "C" void kernel_launch(void* const* d_in, const int* in_sizes, int n_in, void* d_out, int out_size, void* d_ws,
                              size_t ws_size, hipStream_t stream) {
  attn_decode<<<NWG, (NWAVE + 1) * 64, 0, stream>>>((const uint16_t*)d_in[0], (const uint16_t*)d_in[1], (const uint16_t*)d_in[2],
                                                    (__hip_bfloat16*)d_out, (unsigned char*)d_ws);
}
```

```cpp
#include <hip/hip_runtime.h>
#include <hip/hip_bf16.h>
#include <stdint.h>

constexpr int BH = 128, SKV = 4096, D = 128;
#define NWAVE 8
#define U 4
#define SSTEPS 16
#define MAPMODE 1
#define STAMPS 0
#define AUX 2
constexpr int NWG = BH * 2;
constexpr int PB = U * 1024;
constexpr int NP = SKV * D * 2 / PB;
constexpr int SP = SSTEPS * NWAVE;
constexpr int DYN = NP - 2 * SP;
static_assert(DYN >= 0 && SSTEPS >= 2, "");
constexpr unsigned TOK_ENTRY = 0x454e5452u, TOK_READY = 0x52454459u, CLEARED = 0xffffffffu;
constexpr size_t WS_MBOX = (size_t)NWG * 1024;
constexpr size_t WS_CNT = WS_MBOX + (size_t)NWG * 128;
constexpr size_t WS_STAMP = 1 << 20;
constexpr int OOB = 0x7ff00000;

typedef unsigned u32x4 __attribute__((ext_vector_type(4)));
typedef __bf16 bf16x2 __attribute__((ext_vector_type(2)));
typedef float f32x4 __attribute__((ext_vector_type(4)));
typedef float f32x2 __attribute__((ext_vector_type(2)));
#define GAS __attribute__((address_space(1)))
#define LAS __attribute__((address_space(3)))
typedef GAS unsigned gu32;
typedef GAS unsigned long long gu64;
#define RLX_AGENT __ATOMIC_RELAXED, __HIP_MEMORY_SCOPE_AGENT

__device__ __forceinline__ float dot2(unsigned a, unsigned b, float acc) {
  return __builtin_amdgcn_fdot2_f32_bf16(__builtin_bit_cast(bf16x2, a), __builtin_bit_cast(bf16x2, b), acc, false);
}
template <int CTRL> __device__ __forceinline__ float dpp(float x) {
  return __builtin_bit_cast(float, __builtin_amdgcn_mov_dpp(__builtin_bit_cast(int, x), CTRL, 0xf, 0xf, true));
}
__device__ __forceinline__ float row_allsum(float x) {
  x += dpp<0x128>(x); x += dpp<0x124>(x); x += dpp<0x122>(x); x += dpp<0x121>(x);
  return x;
}
__device__ __forceinline__ float xrow_max(float x) {
  auto s = __builtin_amdgcn_permlane16_swap(__float_as_uint(x), __float_as_uint(x), false, false);
  x = fmaxf(__uint_as_float(s[0]), __uint_as_float(s[1]));
  auto t = __builtin_amdgcn_permlane32_swap(__float_as_uint(x), __float_as_uint(x), false, false);
  return fmaxf(__uint_as_float(t[0]), __uint_as_float(t[1]));
}
__device__ __forceinline__ float xrow_sum(float x) {
  auto s = __builtin_amdgcn_permlane16_swap(__float_as_uint(x), __float_as_uint(x), false, false);
  x = __uint_as_float(s[0]) + __uint_as_float(s[1]);
  auto t = __builtin_amdgcn_permlane32_swap(__float_as_uint(x), __float_as_uint(x), false, false);
  return __uint_as_float(t[0]) + __uint_as_float(t[1]);
}
#define SB() __builtin_amdgcn_sched_barrier(0)
#define DRAIN() asm volatile("s_waitcnt vmcnt(0)" ::: "memory")
constexpr unsigned long long TMO = 20000000ull;

struct St { float m, l, acc[8]; u32x4 K[U], V[U]; };

template <int MODE>
__device__ __forceinline__ bool step(St& s, const u32x4& qv, __amdgpu_buffer_rsrc_t rk, __amdgpu_buffer_rsrc_t rv, int voff, int so,
                                     unsigned& tk, gu32* cnt, int lane) {
  constexpr float C2 = 0.088388347648318440f * 1.4426950408889634f;
  float sc[U];
#pragma unroll
  for (int u = 0; u < U; ++u) {
    float d = dot2(s.K[u][0], qv[0], 0.f);
    d = dot2(s.K[u][1], qv[1], d); d = dot2(s.K[u][2], qv[2], d); d = dot2(s.K[u][3], qv[3], d);
    sc[u] = d;
    asm volatile("" : "+v"(sc[u]));
  }
  SB();
  bool more = true;
  if (MODE == 2) {
    const unsigned t = __builtin_amdgcn_readfirstlane(tk);
    more = t < (unsigned)DYN;
    so = more ? (SP + (int)t) * PB : OOB;
  }
  if (MODE >= 1) {
    if (lane == 0) tk = __hip_atomic_fetch_add(cnt, 1u, RLX_AGENT);
    SB();
  }
#pragma unroll
  for (int u = 0; u < U; ++u) s.K[u] = __builtin_amdgcn_raw_buffer_load_b128(rk, voff + u * 1024, so, AUX);
  SB();
#pragma unroll
  for (int u = 0; u < U; ++u) sc[u] = row_allsum(sc[u]) * C2;
  float mn = s.m;
#pragma unroll
  for (int u = 0; u < U; ++u) mn = fmaxf(mn, sc[u]);
  const float alpha = __builtin_amdgcn_exp2f(s.m - mn);
  s.m = mn;
  float p[U], ps = 0.f;
#pragma unroll
  for (int u = 0; u < U; ++u) { p[u] = __builtin_amdgcn_exp2f(sc[u] - mn); ps += p[u]; }
  s.l = fmaf(s.l, alpha, ps);
#pragma unroll
  for (int i = 0; i < 8; ++i) s.acc[i] *= alpha;
#pragma unroll
  for (int u = 0; u < U; ++u) {
#pragma unroll
    for (int j = 0; j < 4; ++j) {
      const unsigned w = s.V[u][j];
      s.acc[2 * j] = fmaf(p[u], __builtin_bit_cast(float, w << 16), s.acc[2 * j]);
      s.acc[2 * j + 1] = fmaf(p[u], __builtin_bit_cast(float, w & 0xffff0000u), s.acc[2 * j + 1]);
    }
  }
  asm volatile("" : "+v"(s.acc[0]), "+v"(s.acc[1]), "+v"(s.acc[2]), "+v"(s.acc[3]), "+v"(s.acc[4]), "+v"(s.acc[5]), "+v"(s.acc[6]), "+v"(s.acc[7]), "+v"(s.l));
  SB();
#pragma unroll
  for (int u = 0; u < U; ++u) s.V[u] = __builtin_amdgcn_raw_buffer_load_b128(rv, voff + u * 1024, so, AUX);
  SB();
  return more;
}

__global__ void __launch_bounds__((NWAVE + 1) * 64) attn_decode(const uint16_t* __restrict__ q, const uint16_t* __restrict__ k,
                                                                const uint16_t* __restrict__ v, __hip_bfloat16* __restrict__ out,
                                                                unsigned char* __restrict__ ws) {
  __shared__ __attribute__((aligned(16))) float lds[NWAVE * 132 + 4];
  const unsigned long long tstart = STAMPS ? __builtin_amdgcn_s_memrealtime() : 0ull;
  int bid = blockIdx.x;
  if (MAPMODE == 1) {
    const int x = bid & 7, i = bid >> 3, j = i >> 3, e = (x & 1) ? (x ^ (2 * j + 1)) : x;
    bid = ((((e >> 1) * 4 + j) * 8 + (i & 7)) << 1) | (x & 1);
  } else if (MAPMODE == 2) {
    const int x = bid & 7, i = bid >> 3, j = i & 3, e = (x & 1) ? (x ^ (2 * j + 1)) : x;
    bid = ((i * 4 + (e >> 1)) << 1) | (x & 1);
  }
  const int bh = bid >> 1, sp = bid & 1;
  const int tid = threadIdx.x, lane = tid & 63, wave = __builtin_amdgcn_readfirstlane(tid >> 6);
  volatile LAS unsigned* flag = (volatile LAS unsigned*)(lds + NWAVE * 132);
  if (__builtin_expect(wave == NWAVE, 0)) {
    if (lane == 0) *flag = 0u;
    __syncthreads();
    gu64* inbox_me = (gu64*)(ws + (size_t)bid * 1024) + lane;
    gu64* inbox_pt = (gu64*)(ws + (size_t)(bid ^ 1) * 1024) + lane;
    gu32* mbox_me = (gu32*)(ws + WS_MBOX + (size_t)bid * 128);
    gu32* mbox_pt = (gu32*)(ws + WS_MBOX + (size_t)(bid ^ 1) * 128);
    gu32* cnt = (gu32*)(ws + WS_CNT + (size_t)bh * 128);
    const unsigned long long t0 = __builtin_amdgcn_s_memrealtime();
    __hip_atomic_store(inbox_me, ~0ull, RLX_AGENT);
    __hip_atomic_store(inbox_me + 64, ~0ull, RLX_AGENT);
    if (lane == 0) __hip_atomic_store(mbox_me, TOK_ENTRY, RLX_AGENT);
    if (lane == 0 && sp == 0) __hip_atomic_store(cnt, 0u, RLX_AGENT);
    DRAIN();
    if (lane == 0) __hip_atomic_store(mbox_pt, TOK_READY, RLX_AGENT);
    if (lane == 0 && sp == 0) *flag = 1u;
    unsigned warm = ((const GAS unsigned*)out)[bh * (D / 2) + lane];
    asm volatile("" :: "v"(warm));
    DRAIN();
    for (bool got = false;;) {
      const unsigned own = __builtin_amdgcn_readfirstlane(__hip_atomic_load(mbox_me, RLX_AGENT));
      const unsigned par = __builtin_amdgcn_readfirstlane(__hip_atomic_load(mbox_pt, RLX_AGENT));
      if (par == TOK_ENTRY) {
        if (lane == 0) __hip_atomic_store(mbox_pt, TOK_READY, RLX_AGENT);
        DRAIN();
      }
      if (got) break;
      if (own == TOK_READY) { got = true; if (lane == 0) *flag = 1u; continue; }
      if (__builtin_amdgcn_s_memrealtime() - t0 > TMO) { if (lane == 0) *flag = 1u; break; }
      __builtin_amdgcn_s_sleep(16);
    }
    __syncthreads();
    const unsigned long long tstream = STAMPS ? __builtin_amdgcn_s_memrealtime() : 0ull;
    unsigned long long x0 = __hip_atomic_load(inbox_me, RLX_AGENT), x1 = __hip_atomic_load(inbox_me + 64, RLX_AGENT);
    float M = -INFINITY;
    f32x2 ml[NWAVE];
#pragma unroll
    for (int w = 0; w < NWAVE; ++w) { ml[w] = *reinterpret_cast<const f32x2*>(lds + w * 132); M = fmaxf(M, ml[w][0]); }
    float L = 0.f, o0 = 0.f, o1 = 0.f;
#pragma unroll
    for (int w = 0; w < NWAVE; ++w) {
      const float wt = __builtin_amdgcn_exp2f(ml[w][0] - M);
      L = fmaf(wt, ml[w][1], L);
      o0 = fmaf(wt, lds[w * 132 + 4 + lane], o0);
      o1 = fmaf(wt, lds[w * 132 + 68 + lane], o1);
    }
    const float inv = 1.0f / L;
    o0 *= inv; o1 *= inv;
    const float lse = M + __builtin_amdgcn_logf(L);
    unsigned tag = __float_as_uint(lse);
    if (tag == CLEARED) tag = 0x7fc00000u;
    __hip_atomic_store(inbox_pt, ((unsigned long long)tag << 32) | __float_as_uint(o0), RLX_AGENT);
    __hip_atomic_store(inbox_pt + 64, ((unsigned long long)tag << 32) | __float_as_uint(o1), RLX_AGENT);
    SB();
    asm volatile("" : "+v"(x0), "+v"(x1));
    bool have = __all((unsigned)(x0 >> 32) != CLEARED && (unsigned)(x1 >> 32) != CLEARED);
    if (!have) {
      DRAIN();
      x0 = __hip_atomic_load(inbox_me, RLX_AGENT); x1 = __hip_atomic_load(inbox_me + 64, RLX_AGENT);
      have = __all((unsigned)(x0 >> 32) != CLEARED && (unsigned)(x1 >> 32) != CLEARED);
    }
    if (have) {
      const float lse_me = __uint_as_float(tag), lse_pt = __uint_as_float((unsigned)(x0 >> 32));
      const float lseA = sp == 0 ? lse_me : lse_pt, lseB = sp == 0 ? lse_pt : lse_me;
      const float p0 = __uint_as_float((unsigned)x0), p1 = __uint_as_float((unsigned)x1);
      const float a0 = sp == 0 ? o0 : p0, b0 = sp == 0 ? p0 : o0, a1 = sp == 0 ? o1 : p1, b1 = sp == 0 ? p1 : o1;
      const float MM = fmaxf(lseA, lseB);
      const float wA = __builtin_amdgcn_exp2f(lseA - MM), wB = __builtin_amdgcn_exp2f(lseB - MM);
      const float iw = 1.0f / (wA + wB);
      out[bh * D + lane] = __float2bfloat16(fmaf(wA, a0, wB * b0) * iw);
      out[bh * D + 64 + lane] = __float2bfloat16(fmaf(wA, a1, wB * b1) * iw);
    }
    if (STAMPS && lane == 0) {
      DRAIN();
      unsigned long long* st = (unsigned long long*)(ws + WS_STAMP) + (size_t)blockIdx.x * 4;
      unsigned xcc; asm volatile("s_getreg_b32 %0, hwreg(HW_REG_XCC_ID)" : "=s"(xcc));
      st[0] = tstart; st[1] = tstream; st[2] = __builtin_amdgcn_s_memrealtime(); st[3] = (xcc & 15) | ((unsigned long long)have << 8) | ((unsigned long long)bid << 16);
    }
    return;
  }
  const int r = lane >> 4, c = lane & 15;
  auto rk = __builtin_amdgcn_make_buffer_rsrc((void*)(k + (size_t)bh * SKV * D), 0, SKV * D * 2, 0x00020000);
  auto rv = __builtin_amdgcn_make_buffer_rsrc((void*)(v + (size_t)bh * SKV * D), 0, SKV * D * 2, 0x00020000);
  const int voff = lane * 16;
  int so = (sp * (NP - SP) + wave) * PB;
  St s;
  const u32x4 qv = *reinterpret_cast<const u32x4*>(q + (size_t)bh * D + 8 * c);
  SB();
#pragma unroll
  for (int u = 0; u < U; ++u) s.K[u] = __builtin_amdgcn_raw_buffer_load_b128(rk, voff + u * 1024, so, AUX);
  SB();
#pragma unroll
  for (int u = 0; u < U; ++u) s.V[u] = __builtin_amdgcn_raw_buffer_load_b128(rv, voff + u * 1024, so, AUX);
  SB();
  __syncthreads();
  s.m = -INFINITY; s.l = 0.f;
#pragma unroll
  for (int i = 0; i < 8; ++i) s.acc[i] = 0.f;
  int zoff;
  asm volatile("v_mov_b32 %0, 0" : "=v"(zoff));
  gu32* cnt = (gu32*)(ws + WS_CNT + (size_t)bh * 128) + zoff;
  unsigned tk = 0;
  for (int i = 0; i < SSTEPS - 2; ++i) { so += NWAVE * PB; step<0>(s, qv, rk, rv, voff, so, tk, cnt, lane); }
  if (DYN > 0) {
    if (*flag == 0u) {
      const unsigned long long t0 = __builtin_amdgcn_s_memrealtime();
      while (*flag == 0u && __builtin_amdgcn_s_memrealtime() - t0 < TMO) __builtin_amdgcn_s_sleep(8);
    }
    so += NWAVE * PB;
    step<1>(s, qv, rk, rv, voff, so, tk, cnt, lane);
    while (step<2>(s, qv, rk, rv, voff, 0, tk, cnt, lane)) {}
  } else {
    so += NWAVE * PB;
    step<0>(s, qv, rk, rv, voff, so, tk, cnt, lane);
    step<0>(s, qv, rk, rv, voff, OOB, tk, cnt, lane);
  }
  const float mw = xrow_max(s.m);
  const float sc = __builtin_amdgcn_exp2f(s.m - mw);
  const float l = xrow_sum(s.l * sc);
#pragma unroll
  for (int i = 0; i < 8; ++i) s.acc[i] = xrow_sum(s.acc[i] * sc);
  float* rec = lds + wave * 132;
  if (r == 0) {
    *reinterpret_cast<f32x4*>(rec + 4 + 8 * c) = f32x4{s.acc[0], s.acc[1], s.acc[2], s.acc[3]};
    *reinterpret_cast<f32x4*>(rec + 8 + 8 * c) = f32x4{s.acc[4], s.acc[5], s.acc[6], s.acc[7]};
    if (c == 0) *reinterpret_cast<f32x2*>(rec) = f32x2{mw, l};
  }
  __syncthreads();
}

extern "C" void kernel_launch(void* const* d_in, const int* in_sizes, int n_in, void* d_out, int out_size, void* d_ws,
                              size_t ws_size, hipStream_t stream) {
  attn_decode<<<NWG, (NWAVE + 1) * 64, 0, stream>>>((const uint16_t*)d_in[0], (const uint16_t*)d_in[1], (const uint16_t*)d_in[2],
                                                    (__hip_bfloat16*)d_out, (unsigned char*)d_ws);
}
```

```cpp
#include <hip/hip_runtime.h>
#include <hip/hip_bf16.h>
#include <stdint.h>
#include <stdio.h>

constexpr int BH = 128, SKV = 4096, D = 128;
#define NWAVE 8
#define U 4
#define SSTEPS 16
#define MAPMODE 1
#define STAMPS 0
#define AUX 2
constexpr int NWG = BH * 2;
constexpr int PB = U * 1024;
constexpr int NP = SKV * D * 2 / PB;
constexpr int SP = SSTEPS * NWAVE;
constexpr int DYN = NP - 2 * SP;
static_assert(DYN >= 0 && SSTEPS >= 2, "");
constexpr unsigned TOK_ENTRY = 0x454e5452u, TOK_READY = 0x52454459u, CLEARED = 0xffffffffu;
constexpr size_t WS_MBOX = (size_t)NWG * 1024;
constexpr size_t WS_CNT = WS_MBOX + (size_t)NWG * 128;
constexpr size_t WS_STAMP = 1 << 20;
constexpr int OOB = 0x7ff00000;

typedef unsigned u32x4 __attribute__((ext_vector_type(4)));
typedef __bf16 bf16x2 __attribute__((ext_vector_type(2)));
typedef float f32x4 __attribute__((ext_vector_type(4)));
typedef float f32x2 __attribute__((ext_vector_type(2)));
#define GAS __attribute__((address_space(1)))
#define LAS __attribute__((address_space(3)))
typedef GAS unsigned gu32;
typedef GAS unsigned long long gu64;
#define RLX_AGENT __ATOMIC_RELAXED, __HIP_MEMORY_SCOPE_AGENT

__device__ __forceinline__ float dot2(unsigned a, unsigned b, float acc) {
  return __builtin_amdgcn_fdot2_f32_bf16(__builtin_bit_cast(bf16x2, a), __builtin_bit_cast(bf16x2, b), acc, false);
}
template <int CTRL> __device__ __forceinline__ float dpp(float x) {
  return __builtin_bit_cast(float, __builtin_amdgcn_mov_dpp(__builtin_bit_cast(int, x), CTRL, 0xf, 0xf, true));
}
__device__ __forceinline__ float row_allsum(float x) {
  x += dpp<0x128>(x); x += dpp<0x124>(x); x += dpp<0x122>(x); x += dpp<0x121>(x);
  return x;
}
__device__ __forceinline__ float xrow_max(float x) {
  auto s = __builtin_amdgcn_permlane16_swap(__float_as_uint(x), __float_as_uint(x), false, false);
  x = fmaxf(__uint_as_float(s[0]), __uint_as_float(s[1]));
  auto t = __builtin_amdgcn_permlane32_swap(__float_as_uint(x), __float_as_uint(x), false, false);
  return fmaxf(__uint_as_float(t[0]), __uint_as_float(t[1]));
}
__device__ __forceinline__ float xrow_sum(float x) {
  auto s = __builtin_amdgcn_permlane16_swap(__float_as_uint(x), __float_as_uint(x), false, false);
  x = __uint_as_float(s[0]) + __uint_as_float(s[1]);
  auto t = __builtin_amdgcn_permlane32_swap(__float_as_uint(x), __float_as_uint(x), false, false);
  return __uint_as_float(t[0]) + __uint_as_float(t[1]);
}
#define SB() __builtin_amdgcn_sched_barrier(0)
#define DRAIN() asm volatile("s_waitcnt vmcnt(0)" ::: "memory")
constexpr unsigned long long TMO = 20000000ull;

struct St { float m, l, acc[8]; u32x4 K[U], V[U]; };

template <int MODE>
__device__ __forceinline__ bool step(St& s, const u32x4& qv, __amdgpu_buffer_rsrc_t rk, __amdgpu_buffer_rsrc_t rv, int voff, int so,
                                     unsigned& tk, gu32* cnt, int lane) {
  constexpr float C2 = 0.088388347648318440f * 1.4426950408889634f;
  float sc[U];
#pragma unroll
  for (int u = 0; u < U; ++u) {
    float d = dot2(s.K[u][0], qv[0], 0.f);
    d = dot2(s.K[u][1], qv[1], d); d = dot2(s.K[u][2], qv[2], d); d = dot2(s.K[u][3], qv[3], d);
    sc[u] = d;
    asm volatile("" : "+v"(sc[u]));
  }
  SB();
  bool more = true;
  if (MODE == 2) {
    const unsigned t = __builtin_amdgcn_readfirstlane(tk);
    more = t < (unsigned)DYN;
    so = more ? (SP + (int)t) * PB : OOB;
  }
  if (MODE >= 1) {
    if (lane == 0) tk = __hip_atomic_fetch_add(cnt, 1u, RLX_AGENT);
    SB();
  }
#pragma unroll
  for (int u = 0; u < U; ++u) s.K[u] = __builtin_amdgcn_raw_buffer_load_b128(rk, voff + u * 1024, so, AUX);
  SB();
#pragma unroll
  for (int u = 0; u < U; ++u) sc[u] = row_allsum(sc[u]) * C2;
  float mn = s.m;
#pragma unroll
  for (int u = 0; u < U; ++u) mn = fmaxf(mn, sc[u]);
  const float alpha = __builtin_amdgcn_exp2f(s.m - mn);
  s.m = mn;
  float p[U], ps = 0.f;
#pragma unroll
  for (int u = 0; u < U; ++u) { p[u] = __builtin_amdgcn_exp2f(sc[u] - mn); ps += p[u]; }
  s.l = fmaf(s.l, alpha, ps);
#pragma unroll
  for (int i = 0; i < 8; ++i) s.acc[i] *= alpha;
#pragma unroll
  for (int u = 0; u < U; ++u) {
#pragma unroll
    for (int j = 0; j < 4; ++j) {
      const unsigned w = s.V[u][j];
      s.acc[2 * j] = fmaf(p[u], __builtin_bit_cast(float, w << 16), s.acc[2 * j]);
      s.acc[2 * j + 1] = fmaf(p[u], __builtin_bit_cast(float, w & 0xffff0000u), s.acc[2 * j + 1]);
    }
  }
  asm volatile("" : "+v"(s.acc[0]), "+v"(s.acc[1]), "+v"(s.acc[2]), "+v"(s.acc[3]), "+v"(s.acc[4]), "+v"(s.acc[5]), "+v"(s.acc[6]), "+v"(s.acc[7]), "+v"(s.l));
  SB();
#pragma unroll
  for (int u = 0; u < U; ++u) s.V[u] = __builtin_amdgcn_raw_buffer_load_b128(rv, voff + u * 1024, so, AUX);
  SB();
  return more;
}

__global__ void __launch_bounds__((NWAVE + 1) * 64) attn_decode(const uint16_t* __restrict__ q, const uint16_t* __restrict__ k,
                                                                const uint16_t* __restrict__ v, __hip_bfloat16* __restrict__ out,
                                                                unsigned char* __restrict__ ws) {
  __shared__ __attribute__((aligned(16))) float lds[NWAVE * 132 + 4];
  const unsigned long long tstart = STAMPS ? __builtin_amdgcn_s_memrealtime() : 0ull;
  int bid = blockIdx.x;
  if (MAPMODE == 1) {
    const int x = bid & 7, i = bid >> 3, j = i >> 3, e = (x & 1) ? (x ^ (2 * j + 1)) : x;
    bid = ((((e >> 1) * 4 + j) * 8 + (i & 7)) << 1) | (x & 1);
  } else if (MAPMODE == 2) {
    const int x = bid & 7, i = bid >> 3, j = i & 3, e = (x & 1) ? (x ^ (2 * j + 1)) : x;
    bid = ((i * 4 + (e >> 1)) << 1) | (x & 1);
  }
  const int bh = bid >> 1, sp = bid & 1;
  const int tid = threadIdx.x, lane = tid & 63, wave = __builtin_amdgcn_readfirstlane(tid >> 6);
  volatile LAS unsigned* flag = (volatile LAS unsigned*)(lds + NWAVE * 132);
  if (__builtin_expect(wave == NWAVE, 0)) {
    if (lane == 0) *flag = 0u;
    __syncthreads();
    gu64* inbox_me = (gu64*)(ws + (size_t)bid * 1024) + lane;
    gu64* inbox_pt = (gu64*)(ws + (size_t)(bid ^ 1) * 1024) + lane;
    gu32* mbox_me = (gu32*)(ws + WS_MBOX + (size_t)bid * 128);
    gu32* mbox_pt = (gu32*)(ws + WS_MBOX + (size_t)(bid ^ 1) * 128);
    gu32* cnt = (gu32*)(ws + WS_CNT + (size_t)bh * 128);
    const unsigned long long t0 = __builtin_amdgcn_s_memrealtime();
    __hip_atomic_store(inbox_me, ~0ull, RLX_AGENT);
    __hip_atomic_store(inbox_me + 64, ~0ull, RLX_AGENT);
    if (lane == 0) __hip_atomic_store(mbox_me, TOK_ENTRY, RLX_AGENT);
    if (lane == 0 && sp == 0) __hip_atomic_store(cnt, 0u, RLX_AGENT);
    DRAIN();
    if (lane == 0) __hip_atomic_store(mbox_pt, TOK_READY, RLX_AGENT);
    if (lane == 0 && sp == 0) *flag = 1u;
    unsigned warm = ((const GAS unsigned*)out)[bh * (D / 2) + lane];
    asm volatile("" :: "v"(warm));
    DRAIN();
    for (bool got = false;;) {
      const unsigned own = __builtin_amdgcn_readfirstlane(__hip_atomic_load(mbox_me, RLX_AGENT));
      const unsigned par = __builtin_amdgcn_readfirstlane(__hip_atomic_load(mbox_pt, RLX_AGENT));
      if (par == TOK_ENTRY) {
        if (lane == 0) __hip_atomic_store(mbox_pt, TOK_READY, RLX_AGENT);
        DRAIN();
      }
      if (got) break;
      if (own == TOK_READY) { got = true; if (lane == 0) *flag = 1u; continue; }
      if (__builtin_amdgcn_s_memrealtime() - t0 > TMO) { if (lane == 0) *flag = 1u; break; }
      __builtin_amdgcn_s_sleep(16);
    }
    __syncthreads();
    const unsigned long long tstream = STAMPS ? __builtin_amdgcn_s_memrealtime() : 0ull;
    unsigned long long x0 = __hip_atomic_load(inbox_me, RLX_AGENT), x1 = __hip_atomic_load(inbox_me + 64, RLX_AGENT);
    float M = -INFINITY;
    f32x2 ml[NWAVE];
#pragma unroll
    for (int w = 0; w < NWAVE; ++w) { ml[w] = *reinterpret_cast<const f32x2*>(lds + w * 132); M = fmaxf(M, ml[w][0]); }
    float L = 0.f, o0 = 0.f, o1 = 0.f;
#pragma unroll
    for (int w = 0; w < NWAVE; ++w) {
      const float wt = __builtin_amdgcn_exp2f(ml[w][0] - M);
      L = fmaf(wt, ml[w][1], L);
      o0 = fmaf(wt, lds[w * 132 + 4 + lane], o0);
      o1 = fmaf(wt, lds[w * 132 + 68 + lane], o1);
    }
    const float inv = 1.0f / L;
    o0 *= inv; o1 *= inv;
    const float lse = M + __builtin_amdgcn_logf(L);
    unsigned tag = __float_as_uint(lse);
    if (tag == CLEARED) tag = 0x7fc00000u;
    __hip_atomic_store(inbox_pt, ((unsigned long long)tag << 32) | __float_as_uint(o0), RLX_AGENT);
    __hip_atomic_store(inbox_pt + 64, ((unsigned long long)tag << 32) | __float_as_uint(o1), RLX_AGENT);
    SB();
    asm volatile("" : "+v"(x0), "+v"(x1));
    bool have = __all((unsigned)(x0 >> 32) != CLEARED && (unsigned)(x1 >> 32) != CLEARED);
    if (!have) {
      DRAIN();
      x0 = __hip_atomic_load(inbox_me, RLX_AGENT); x1 = __hip_atomic_load(inbox_me + 64, RLX_AGENT);
      have = __all((unsigned)(x0 >> 32) != CLEARED && (unsigned)(x1 >> 32) != CLEARED);
    }
    if (have) {
      const float lse_me = __uint_as_float(tag), lse_pt = __uint_as_float((unsigned)(x0 >> 32));
      const float lseA = sp == 0 ? lse_me : lse_pt, lseB = sp == 0 ? lse_pt : lse_me;
      const float p0 = __uint_as_float((unsigned)x0), p1 = __uint_as_float((unsigned)x1);
      const float a0 = sp == 0 ? o0 : p0, b0 = sp == 0 ? p0 : o0, a1 = sp == 0 ? o1 : p1, b1 = sp == 0 ? p1 : o1;
      const float MM = fmaxf(lseA, lseB);
      const float wA = __builtin_amdgcn_exp2f(lseA - MM), wB = __builtin_amdgcn_exp2f(lseB - MM);
      const float iw = 1.0f / (wA + wB);
      out[bh * D + lane] = __float2bfloat16(fmaf(wA, a0, wB * b0) * iw);
      out[bh * D + 64 + lane] = __float2bfloat16(fmaf(wA, a1, wB * b1) * iw);
    }
    if (STAMPS && lane == 0) {
      DRAIN();
      unsigned long long* st = (unsigned long long*)(ws + WS_STAMP) + (size_t)blockIdx.x * 4;
      unsigned xcc; asm volatile("s_getreg_b32 %0, hwreg(HW_REG_XCC_ID)" : "=s"(xcc));
      st[0] = tstart; st[1] = tstream; st[2] = __builtin_amdgcn_s_memrealtime(); st[3] = (xcc & 15) | ((unsigned long long)have << 8) | ((unsigned long long)bid << 16);
    }
    return;
  }
  const int r = lane >> 4, c = lane & 15;
  auto rk = __builtin_amdgcn_make_buffer_rsrc((void*)(k + (size_t)bh * SKV * D), 0, SKV * D * 2, 0x00020000);
  auto rv = __builtin_amdgcn_make_buffer_rsrc((void*)(v + (size_t)bh * SKV * D), 0, SKV * D * 2, 0x00020000);
  const int voff = lane * 16;
  int so = (sp * (NP - SP) + wave) * PB;
  St s;
  const u32x4 qv = *reinterpret_cast<const u32x4*>(q + (size_t)bh * D + 8 * c);
  SB();
#pragma unroll
  for (int u = 0; u < U; ++u) s.K[u] = __builtin_amdgcn_raw_buffer_load_b128(rk, voff + u * 1024, so, AUX);
  SB();
#pragma unroll
  for (int u = 0; u < U; ++u) s.V[u] = __builtin_amdgcn_raw_buffer_load_b128(rv, voff + u * 1024, so, AUX);
  SB();
  __syncthreads();
  s.m = -INFINITY; s.l = 0.f;
#pragma unroll
  for (int i = 0; i < 8; ++i) s.acc[i] = 0.f;
  int zoff;
  asm volatile("v_mov_b32 %0, 0" : "=v"(zoff));
  gu32* cnt = (gu32*)(ws + WS_CNT + (size_t)bh * 128) + zoff;
  unsigned tk = 0;
  for (int i = 0; i < SSTEPS - 2; ++i) { so += NWAVE * PB; step<0>(s, qv, rk, rv, voff, so, tk, cnt, lane); }
  if (DYN > 0) {
    if (*flag == 0u) {
      const unsigned long long t0 = __builtin_amdgcn_s_memrealtime();
      while (*flag == 0u && __builtin_amdgcn_s_memrealtime() - t0 < TMO) __builtin_amdgcn_s_sleep(8);
    }
    so += NWAVE * PB;
    step<1>(s, qv, rk, rv, voff, so, tk, cnt, lane);
    while (step<2>(s, qv, rk, rv, voff, 0, tk, cnt, lane)) {}
  } else {
    so += NWAVE * PB;
    step<0>(s, qv, rk, rv, voff, so, tk, cnt, lane);
    step<0>(s, qv, rk, rv, voff, OOB, tk, cnt, lane);
  }
  const float mw = xrow_max(s.m);
  const float sc = __builtin_amdgcn_exp2f(s.m - mw);
  const float l = xrow_sum(s.l * sc);
#pragma unroll
  for (int i = 0; i < 8; ++i) s.acc[i] = xrow_sum(s.acc[i] * sc);
  float* rec = lds + wave * 132;
  if (r == 0) {
    *reinterpret_cast<f32x4*>(rec + 4 + 8 * c) = f32x4{s.acc[0], s.acc[1], s.acc[2], s.acc[3]};
    *reinterpret_cast<f32x4*>(rec + 8 + 8 * c) = f32x4{s.acc[4], s.acc[5], s.acc[6], s.acc[7]};
    if (c == 0) *reinterpret_cast<f32x2*>(rec) = f32x2{mw, l};
  }
  __syncthreads();
}

extern "C" void kernel_launch(void* const* d_in, const int* in_sizes, int n_in, void* d_out, int out_size, void* d_ws,
                              size_t ws_size, hipStream_t stream) {
  {
    void* ps[5] = {d_in[0], d_in[1], d_in[2], d_out, d_ws};
    for (int i = 0; i < 5; ++i) {
      hipDeviceptr_t base = 0; size_t sz = 0;
      hipError_t e = hipMemGetAddressRange(&base, &sz, (hipDeviceptr_t)ps[i]);
      hipPointerAttribute_t at; hipError_t e2 = hipPointerGetAttributes(&at, ps[i]);
      fprintf(stderr, "PTR %d: %p base %p size %zu (err %d) type %d managed %d flags %u (err %d)\n", i, ps[i], (void*)base, sz, (int)e, (int)at.type, at.isManaged, at.allocationFlags, (int)e2);
      printf("PTR %d: %p base %p size %zu (err %d) type %d flags %u\n", i, ps[i], (void*)base, sz, (int)e, (int)at.type, at.allocationFlags);
    }
  }
  attn_decode<<<NWG, (NWAVE + 1) * 64, 0, stream>>>((const uint16_t*)d_in[0], (const uint16_t*)d_in[1], (const uint16_t*)d_in[2],
                                                    (__hip_bfloat16*)d_out, (unsigned char*)d_ws);
}
```

```cpp
#include <hip/hip_runtime.h>
#include <hip/hip_bf16.h>
#include <stdint.h>

constexpr int BH = 128, SKV = 4096, D = 128;
constexpr int NWG = BH * 2;
constexpr unsigned TOK_ENTRY = 0x454e5452u, TOK_READY = 0x52454459u, CLEARED = 0xffffffffu;
constexpr size_t WS_MBOX = (size_t)NWG * 1024;
constexpr size_t WS_STAMP = 1 << 20;
constexpr int OOB = 0x7ff00000;
#define AUX 2

typedef unsigned u32x4 __attribute__((ext_vector_type(4)));
typedef __bf16 bf16x2 __attribute__((ext_vector_type(2)));
typedef float f32x4 __attribute__((ext_vector_type(4)));
typedef float f32x2 __attribute__((ext_vector_type(2)));
#define GAS __attribute__((address_space(1)))
#define LAS __attribute__((address_space(3)))
typedef GAS unsigned gu32;
typedef GAS unsigned long long gu64;
#define RLX_AGENT __ATOMIC_RELAXED, __HIP_MEMORY_SCOPE_AGENT

__device__ __forceinline__ float dot2(unsigned a, unsigned b, float acc) {
  return __builtin_amdgcn_fdot2_f32_bf16(__builtin_bit_cast(bf16x2, a), __builtin_bit_cast(bf16x2, b), acc, false);
}
template <int CTRL> __device__ __forceinline__ float dpp(float x) {
  return __builtin_bit_cast(float, __builtin_amdgcn_mov_dpp(__builtin_bit_cast(int, x), CTRL, 0xf, 0xf, true));
}
__device__ __forceinline__ float row_allsum(float x) {
  x += dpp<0x128>(x); x += dpp<0x124>(x); x += dpp<0x122>(x); x += dpp<0x121>(x);
  return x;
}
__device__ __forceinline__ float row_allmax(float x) {
  x = fmaxf(x, dpp<0x128>(x)); x = fmaxf(x, dpp<0x124>(x)); x = fmaxf(x, dpp<0x122>(x)); x = fmaxf(x, dpp<0x121>(x));
  return x;
}
__device__ __forceinline__ float xrow_max(float x) {
  auto s = __builtin_amdgcn_permlane16_swap(__float_as_uint(x), __float_as_uint(x), false, false);
  x = fmaxf(__uint_as_float(s[0]), __uint_as_float(s[1]));
  auto t = __builtin_amdgcn_permlane32_swap(__float_as_uint(x), __float_as_uint(x), false, false);
  return fmaxf(__uint_as_float(t[0]), __uint_as_float(t[1]));
}
__device__ __forceinline__ float xrow_sum(float x) {
  auto s = __builtin_amdgcn_permlane16_swap(__float_as_uint(x), __float_as_uint(x), false, false);
  x = __uint_as_float(s[0]) + __uint_as_float(s[1]);
  auto t = __builtin_amdgcn_permlane32_swap(__float_as_uint(x), __float_as_uint(x), false, false);
  return __uint_as_float(t[0]) + __uint_as_float(t[1]);
}
#define SB() __builtin_amdgcn_sched_barrier(0)
#define DRAIN() asm volatile("s_waitcnt vmcnt(0)" ::: "memory")
constexpr unsigned long long TMO = 20000000ull;
constexpr float C2 = 0.088388347648318440f * 1.4426950408889634f;

template <int MAP> __device__ __forceinline__ int map_bid(int bid) {
  if (MAP == 1) {
    const int x = bid & 7, i = bid >> 3, j = i >> 3, e = (x & 1) ? (x ^ (2 * j + 1)) : x;
    return ((((e >> 1) * 4 + j) * 8 + (i & 7)) << 1) | (x & 1);
  } else if (MAP == 2) {
    return (bid & 7) * (NWG / 8) + (bid >> 3);
  }
  else if (MAP == 3) { const int x = bid & 7, i = bid >> 3; return (((x & 3) * 32 + i) << 1) | (x >> 2); }
  return bid;
}

template <int NW, bool STMP, int PV = 0>
__device__ __forceinline__ void protocol(__hip_bfloat16* __restrict__ out, unsigned char* __restrict__ ws, const float* lds, int bid, int lane,
                                         unsigned long long tstart) {
  const int bh = bid >> 1, sp = bid & 1;
  gu64* inbox_me = (gu64*)(ws + (size_t)bid * 1024) + lane;
  gu64* inbox_pt = (gu64*)(ws + (size_t)(bid ^ 1) * 1024) + lane;
  gu32* mbox_me = (gu32*)(ws + WS_MBOX + (size_t)bid * 128);
  gu32* mbox_pt = (gu32*)(ws + WS_MBOX + (size_t)(bid ^ 1) * 128);
  if (PV == 1 || PV == 4) { while (__builtin_amdgcn_s_memrealtime() - tstart < (PV == 1 ? 800ull : 2000ull)) __builtin_amdgcn_s_sleep(32); }
  if (PV != 3) {
  const unsigned long long t0 = __builtin_amdgcn_s_memrealtime();
  __hip_atomic_store(inbox_me, ~0ull, RLX_AGENT);
  __hip_atomic_store(inbox_me + 64, ~0ull, RLX_AGENT);
  if (lane == 0) __hip_atomic_store(mbox_me, TOK_ENTRY, RLX_AGENT);
  DRAIN();
  if (lane == 0) __hip_atomic_store(mbox_pt, TOK_READY, RLX_AGENT);
  if (PV != 2) { unsigned warm = ((const GAS unsigned*)out)[bh * (D / 2) + lane];
  asm volatile("" :: "v"(warm)); }
  DRAIN();
  for (bool got = false;;) {
    const unsigned own = __builtin_amdgcn_readfirstlane(__hip_atomic_load(mbox_me, RLX_AGENT));
    const unsigned par = __builtin_amdgcn_readfirstlane(__hip_atomic_load(mbox_pt, RLX_AGENT));
    if (par == TOK_ENTRY) {
      if (lane == 0) __hip_atomic_store(mbox_pt, TOK_READY, RLX_AGENT);
      DRAIN();
    }
    if (got) break;
    if (own == TOK_READY) { got = true; continue; }
    if (__builtin_amdgcn_s_memrealtime() - t0 > TMO) break;
    __builtin_amdgcn_s_sleep(16);
  }
  }
  __syncthreads();
  const unsigned long long tstream = STMP ? __builtin_amdgcn_s_memrealtime() : 0ull;
  unsigned long long x0 = __hip_atomic_load(inbox_me, RLX_AGENT), x1 = __hip_atomic_load(inbox_me + 64, RLX_AGENT);
  float M = -INFINITY;
  f32x2 ml[NW];
#pragma unroll
  for (int w = 0; w < NW; ++w) { ml[w] = *reinterpret_cast<const f32x2*>(lds + w * 132); M = fmaxf(M, ml[w][0]); }
  float L = 0.f, o0 = 0.f, o1 = 0.f;
#pragma unroll
  for (int w = 0; w < NW; ++w) {
    const float wt = __builtin_amdgcn_exp2f(ml[w][0] - M);
    L = fmaf(wt, ml[w][1], L);
    o0 = fmaf(wt, lds[w * 132 + 4 + lane], o0);
    o1 = fmaf(wt, lds[w * 132 + 68 + lane], o1);
  }
  const float inv = 1.0f / L;
  o0 *= inv; o1 *= inv;
  const float lse = M + __builtin_amdgcn_logf(L);
  unsigned tag = __float_as_uint(lse);
  if (tag == CLEARED) tag = 0x7fc00000u;
  __hip_atomic_store(inbox_pt, ((unsigned long long)tag << 32) | __float_as_uint(o0), RLX_AGENT);
  __hip_atomic_store(inbox_pt + 64, ((unsigned long long)tag << 32) | __float_as_uint(o1), RLX_AGENT);
  SB();
  asm volatile("" : "+v"(x0), "+v"(x1));
  bool have = __all((unsigned)(x0 >> 32) != CLEARED && (unsigned)(x1 >> 32) != CLEARED);
  if (!have) {
    DRAIN();
    x0 = __hip_atomic_load(inbox_me, RLX_AGENT); x1 = __hip_atomic_load(inbox_me + 64, RLX_AGENT);
    have = __all((unsigned)(x0 >> 32) != CLEARED && (unsigned)(x1 >> 32) != CLEARED);
  }
  if (have) {
    const float lse_me = __uint_as_float(tag), lse_pt = __uint_as_float((unsigned)(x0 >> 32));
    const float lseA = sp == 0 ? lse_me : lse_pt, lseB = sp == 0 ? lse_pt : lse_me;
    const float p0 = __uint_as_float((unsigned)x0), p1 = __uint_as_float((unsigned)x1);
    const float a0 = sp == 0 ? o0 : p0, b0 = sp == 0 ? p0 : o0, a1 = sp == 0 ? o1 : p1, b1 = sp == 0 ? p1 : o1;
    const float MM = fmaxf(lseA, lseB);
    const float wA = __builtin_amdgcn_exp2f(lseA - MM), wB = __builtin_amdgcn_exp2f(lseB - MM);
    const float iw = 1.0f / (wA + wB);
    out[bh * D + lane] = __float2bfloat16(fmaf(wA, a0, wB * b0) * iw);
    out[bh * D + 64 + lane] = __float2bfloat16(fmaf(wA, a1, wB * b1) * iw);
  }
  if (STMP && lane == 0) {
    DRAIN();
    unsigned long long* st = (unsigned long long*)(ws + WS_STAMP) + (size_t)blockIdx.x * 4;
    unsigned xcc; asm volatile("s_getreg_b32 %0, hwreg(HW_REG_XCC_ID)" : "=s"(xcc));
    st[0] = tstart; st[1] = tstream; st[2] = __builtin_amdgcn_s_memrealtime(); st[3] = (xcc & 15) | ((unsigned long long)have << 8) | ((unsigned long long)bid << 16);
  }
}

__device__ __forceinline__ void write_record(float* rec, float mw, float l, float (&acc)[8], int r, int c) {
  if (r == 0) {
    *reinterpret_cast<f32x4*>(rec + 4 + 8 * c) = f32x4{acc[0], acc[1], acc[2], acc[3]};
    *reinterpret_cast<f32x4*>(rec + 8 + 8 * c) = f32x4{acc[4], acc[5], acc[6], acc[7]};
    if (c == 0) *reinterpret_cast<f32x2*>(rec) = f32x2{mw, l};
  }
}

template <int NW, int U, int RING, int MAP, bool STMP>
__global__ void __launch_bounds__((NW + 1) * 64) attn_online(const uint16_t* __restrict__ q, const uint16_t* __restrict__ k, const uint16_t* __restrict__ v,
                                                             __hip_bfloat16* __restrict__ out, unsigned char* __restrict__ ws) {
  __shared__ __attribute__((aligned(16))) float lds[NW * 132];
  const unsigned long long tstart = STMP ? __builtin_amdgcn_s_memrealtime() : 0ull;
  const int bid = map_bid<MAP>(blockIdx.x);
  const int bh = bid >> 1;
  const int tid = threadIdx.x, lane = tid & 63, wave = __builtin_amdgcn_readfirstlane(tid >> 6);
  if (__builtin_expect(wave == NW, 0)) { protocol<NW, STMP>(out, ws, lds, bid, lane, tstart); return; }
  constexpr int STEPS = 2048 / (NW * 4 * U);
  const int r = lane >> 4, c = lane & 15;
  auto rk = __builtin_amdgcn_make_buffer_rsrc((void*)(k + (size_t)bid * 2048 * D), 0, 2048 * D * 2, 0x00020000);
  auto rv = __builtin_amdgcn_make_buffer_rsrc((void*)(v + (size_t)bid * 2048 * D), 0, 2048 * D * 2, 0x00020000);
  const int voff = lane * 16;
  const u32x4 qv = *reinterpret_cast<const u32x4*>(q + (size_t)bh * D + 8 * c);
  SB();
  u32x4 Kr[RING][U], Vr[RING][U];
#pragma unroll
  for (int g = 0; g < RING; ++g) {
    const int so = (g * NW + wave) * U * 1024;
#pragma unroll
    for (int u = 0; u < U; ++u) Kr[g][u] = __builtin_amdgcn_raw_buffer_load_b128(rk, voff + u * 1024, so, AUX);
    SB();
#pragma unroll
    for (int u = 0; u < U; ++u) Vr[g][u] = __builtin_amdgcn_raw_buffer_load_b128(rv, voff + u * 1024, so, AUX);
    SB();
  }
  float m = -INFINITY, l = 0.f, acc[8];
#pragma unroll
  for (int i = 0; i < 8; ++i) acc[i] = 0.f;
  for (int s0 = 0; s0 < STEPS; s0 += RING) {
#pragma unroll
    for (int g = 0; g < RING; ++g) {
      const int so = ((s0 + g + RING) * NW + wave) * U * 1024;
      float sc[U];
#pragma unroll
      for (int u = 0; u < U; ++u) {
        float d = dot2(Kr[g][u][0], qv[0], 0.f);
        d = dot2(Kr[g][u][1], qv[1], d); d = dot2(Kr[g][u][2], qv[2], d); d = dot2(Kr[g][u][3], qv[3], d);
        sc[u] = d;
        asm volatile("" : "+v"(sc[u]));
      }
      SB();
#pragma unroll
      for (int u = 0; u < U; ++u) Kr[g][u] = __builtin_amdgcn_raw_buffer_load_b128(rk, voff + u * 1024, so, AUX);
      SB();
#pragma unroll
      for (int u = 0; u < U; ++u) sc[u] = row_allsum(sc[u]) * C2;
      float mn = m;
#pragma unroll
      for (int u = 0; u < U; ++u) mn = fmaxf(mn, sc[u]);
      const float alpha = __builtin_amdgcn_exp2f(m - mn);
      m = mn;
      float p[U], ps = 0.f;
#pragma unroll
      for (int u = 0; u < U; ++u) { p[u] = __builtin_amdgcn_exp2f(sc[u] - mn); ps += p[u]; }
      l = fmaf(l, alpha, ps);
#pragma unroll
      for (int i = 0; i < 8; ++i) acc[i] *= alpha;
#pragma unroll
      for (int u = 0; u < U; ++u) {
#pragma unroll
        for (int j = 0; j < 4; ++j) {
          const unsigned w = Vr[g][u][j];
          acc[2 * j] = fmaf(p[u], __builtin_bit_cast(float, w << 16), acc[2 * j]);
          acc[2 * j + 1] = fmaf(p[u], __builtin_bit_cast(float, w & 0xffff0000u), acc[2 * j + 1]);
        }
      }
      asm volatile("" : "+v"(acc[0]), "+v"(acc[1]), "+v"(acc[2]), "+v"(acc[3]), "+v"(acc[4]), "+v"(acc[5]), "+v"(acc[6]), "+v"(acc[7]), "+v"(l));
      SB();
#pragma unroll
      for (int u = 0; u < U; ++u) Vr[g][u] = __builtin_amdgcn_raw_buffer_load_b128(rv, voff + u * 1024, so, AUX);
      SB();
    }
  }
  const float mw = xrow_max(m);
  const float sc = __builtin_amdgcn_exp2f(m - mw);
  l = xrow_sum(l * sc);
#pragma unroll
  for (int i = 0; i < 8; ++i) acc[i] = xrow_sum(acc[i] * sc);
  write_record(lds + wave * 132, mw, l, acc, r, c);
  __syncthreads();
}

template <int NW, int U, int NG, int MAP, bool STMP>
__global__ void __launch_bounds__((NW + 1) * 64) attn_2ph(const uint16_t* __restrict__ q, const uint16_t* __restrict__ k, const uint16_t* __restrict__ v,
                                                          __hip_bfloat16* __restrict__ out, unsigned char* __restrict__ ws) {
  constexpr int NS = 2048 / (NW * 4 * U);
  constexpr int SB16 = (NS + 15) / 16;
  static_assert(NS % NG == 0 && NS >= 2 * NG && (U % 4 == 0 || U == 2), "");
  __shared__ __attribute__((aligned(16))) float lds[NW * 132 + 2048];
  const unsigned long long tstart = STMP ? __builtin_amdgcn_s_memrealtime() : 0ull;
  const int bid = map_bid<MAP>(blockIdx.x);
  const int bh = bid >> 1;
  const int tid = threadIdx.x, lane = tid & 63, wave = __builtin_amdgcn_readfirstlane(tid >> 6);
  if (__builtin_expect(wave == NW, 0)) { protocol<NW, STMP>(out, ws, lds, bid, lane, tstart); return; }
  const int r = lane >> 4, c = lane & 15;
  auto rk = __builtin_amdgcn_make_buffer_rsrc((void*)(k + (size_t)bid * 2048 * D), 0, 2048 * D * 2, 0x00020000);
  auto rv = __builtin_amdgcn_make_buffer_rsrc((void*)(v + (size_t)bid * 2048 * D), 0, 2048 * D * 2, 0x00020000);
  const int voff = lane * 16;
  const u32x4 qv = *reinterpret_cast<const u32x4*>(q + (size_t)bh * D + 8 * c);
  SB();
  u32x4 R[NG][U];
#pragma unroll
  for (int g = 0; g < NG; ++g) {
#pragma unroll
    for (int u = 0; u < U; ++u) R[g][u] = __builtin_amdgcn_raw_buffer_load_b128(rk, voff + u * 1024, (g * NW + wave) * U * 1024, AUX);
    SB();
  }
  float S[SB16][U];
#pragma unroll
  for (int b = 0; b < SB16; ++b)
#pragma unroll
    for (int u = 0; u < U; ++u) S[b][u] = -INFINITY;
  auto kstep = [&](int g, int b, int s, __amdgpu_buffer_rsrc_t rn, int so) {
    float sc[U];
#pragma unroll
    for (int u = 0; u < U; ++u) {
      float d = dot2(R[g][u][0], qv[0], 0.f);
      d = dot2(R[g][u][1], qv[1], d); d = dot2(R[g][u][2], qv[2], d); d = dot2(R[g][u][3], qv[3], d);
      sc[u] = d;
      asm volatile("" : "+v"(sc[u]));
    }
    SB();
#pragma unroll
    for (int u = 0; u < U; ++u) R[g][u] = __builtin_amdgcn_raw_buffer_load_b128(rn, voff + u * 1024, so, AUX);
    SB();
    const bool mine = c == (s & 15);
#pragma unroll
    for (int u = 0; u < U; ++u) { const float t = row_allsum(sc[u]); S[b][u] = mine ? t : S[b][u]; asm volatile("" : "+v"(S[b][u])); }
  };
#pragma unroll
  for (int b = 0; b < SB16; ++b) {
    const int lo = b * 16, hi = (b + 1) * 16 < NS - NG ? (b + 1) * 16 : NS - NG;
    for (int s0 = lo; s0 < hi; s0 += NG) {
#pragma unroll
      for (int g = 0; g < NG; ++g) kstep(g, b, s0 + g, rk, ((s0 + g + NG) * NW + wave) * U * 1024);
    }
  }
#pragma unroll
  for (int g = 0; g < NG; ++g) kstep(g, SB16 - 1, NS - NG + g, rv, (g * NW + wave) * U * 1024);
  float mw = -INFINITY;
#pragma unroll
  for (int b = 0; b < SB16; ++b)
#pragma unroll
    for (int u = 0; u < U; ++u) mw = fmaxf(mw, S[b][u]);
  mw = xrow_max(row_allmax(mw)) * C2;
  float l = 0.f;
  float* pl = lds + NW * 132 + wave * (2048 / NW);
#pragma unroll
  for (int b = 0; b < SB16; ++b) {
#pragma unroll
    for (int u = 0; u < U; ++u) { S[b][u] = __builtin_amdgcn_exp2f(fmaf(S[b][u], C2, -mw)); l += S[b][u]; }
    if (NS >= 16 || c < NS) {
      float* dst = pl + ((b * 16 + c) * 4 + r) * U;
      if (U == 2) *reinterpret_cast<f32x2*>(dst) = f32x2{S[b][0], S[b][1]};
      else {
#pragma unroll
        for (int u = 0; u < U; u += 4) *reinterpret_cast<f32x4*>(dst + u) = f32x4{S[b][u], S[b][u + 1], S[b][u + 2], S[b][u + 3]};
      }
    }
  }
  asm volatile("" ::: "memory");
  l = xrow_sum(row_allsum(l));
  float acc[8];
#pragma unroll
  for (int i = 0; i < 8; ++i) acc[i] = 0.f;
  const float* pr = pl + r * U;
  for (int s0 = 0; s0 < NS; s0 += NG) {
#pragma unroll
    for (int g = 0; g < NG; ++g) {
      const int so = ((s0 + g + NG) * NW + wave) * U * 1024;
      float p[U];
      if (U == 2) { const f32x2 t = *reinterpret_cast<const f32x2*>(pr + (s0 + g) * 4 * U); p[0] = t[0]; p[1] = t[1]; }
      else {
#pragma unroll
        for (int u = 0; u < U; u += 4) { const f32x4 t = *reinterpret_cast<const f32x4*>(pr + (s0 + g) * 4 * U + u); p[u] = t[0]; p[u + 1] = t[1]; p[u + 2] = t[2]; p[u + 3] = t[3]; }
      }
#pragma unroll
      for (int u = 0; u < U; ++u) {
#pragma unroll
        for (int j = 0; j < 4; ++j) {
          const unsigned w = R[g][u][j];
          acc[2 * j] = fmaf(p[u], __builtin_bit_cast(float, w << 16), acc[2 * j]);
          acc[2 * j + 1] = fmaf(p[u], __builtin_bit_cast(float, w & 0xffff0000u), acc[2 * j + 1]);
        }
      }
      asm volatile("" : "+v"(acc[0]), "+v"(acc[1]), "+v"(acc[2]), "+v"(acc[3]), "+v"(acc[4]), "+v"(acc[5]), "+v"(acc[6]), "+v"(acc[7]));
      SB();
#pragma unroll
      for (int u = 0; u < U; ++u) R[g][u] = __builtin_amdgcn_raw_buffer_load_b128(rv, voff + u * 1024, so, AUX);
      SB();
    }
  }
#pragma unroll
  for (int i = 0; i < 8; ++i) acc[i] = xrow_sum(acc[i]);
  write_record(lds + wave * 132, mw, l, acc, r, c);
  __syncthreads();
}

template <int NW, int U, int NG, int NGM, int PACE>
__device__ __forceinline__ void stream2(const uint16_t* __restrict__ q, const uint16_t* __restrict__ k, const uint16_t* __restrict__ v, float* lds,
                                        int bid, int lane, int wave) {
  constexpr int NS = 2048 / (NW * 4 * U);
  constexpr int KM = ((NS - NG) / NG) * NG;
  constexpr int PW = (NS + NGM) * 4 * U;
  constexpr int GB = NW * U * 1024;
  const int bh = bid >> 1;
  const unsigned tw0 = PACE ? (unsigned)__builtin_amdgcn_s_memrealtime() : 0u;
  auto pace = [&](int n) {
    if (PACE) { const unsigned due = (unsigned)(n * PACE / (2 * NS)); while ((unsigned)__builtin_amdgcn_s_memrealtime() - tw0 < due) __builtin_amdgcn_s_sleep(1); }
  };
  const int r = lane >> 4, c = lane & 15;
  auto rk = __builtin_amdgcn_make_buffer_rsrc((void*)(k + (size_t)bid * 2048 * D), 0, 2048 * D * 2, 0x00020000);
  auto rv = __builtin_amdgcn_make_buffer_rsrc((void*)(v + (size_t)bid * 2048 * D), 0, 2048 * D * 2, 0x00020000);
  const int voff = lane * 16;
  const int wo = wave * U * 1024;
  const u32x4 qv = *reinterpret_cast<const u32x4*>(q + (size_t)bh * D + 8 * c);
  SB();
  u32x4 R[NG][U];
#pragma unroll
  for (int g = 0; g < NG; ++g) {
#pragma unroll
    for (int u = 0; u < U; ++u) R[g][u] = __builtin_amdgcn_raw_buffer_load_b128(rk, voff + u * 1024, wo + g * GB, AUX);
    SB();
  }
  float* pl = lds + NW * 132 + wave * PW;
  float* pw = pl + r * U + (c & (U - 1));
  float mx = -INFINITY;
  auto kstep = [&](int g, int s, __amdgpu_buffer_rsrc_t rn, int so) {
    float sc[U];
#pragma unroll
    for (int u = 0; u < U; ++u) {
      float d = dot2(R[g][u][0], qv[0], 0.f);
      d = dot2(R[g][u][1], qv[1], d); d = dot2(R[g][u][2], qv[2], d); d = dot2(R[g][u][3], qv[3], d);
      sc[u] = d;
      asm volatile("" : "+v"(sc[u]));
    }
    SB();
    pace(s + NG);
#pragma unroll
    for (int u = 0; u < U; ++u) R[g][u] = __builtin_amdgcn_raw_buffer_load_b128(rn, voff + u * 1024, so, AUX);
    SB();
    float t = row_allsum(sc[0]);
#pragma unroll
    for (int u = 1; u < U; ++u) { const float tu = row_allsum(sc[u]); t = (c & (U - 1)) == u ? tu : t; }
    mx = fmaxf(mx, t);
    if (c < U) pw[s * 4 * U] = t;
    asm volatile("" : "+v"(mx));
  };
  for (int s0 = 0; s0 < KM; s0 += NG) {
#pragma unroll
    for (int g = 0; g < NG; ++g) kstep(g, s0 + g, rk, wo + (s0 + g + NG) * GB);
  }
#pragma unroll
  for (int s = KM; s < NS; ++s) {
    if (s + NG < NS) kstep((s - KM) % NG, s, rk, wo + (s + NG) * GB);
    else kstep((s - KM) % NG, s, rv, wo + (s + NG - NS) * GB);
  }
  asm volatile("" ::: "memory");
  const float mw = xrow_max(row_allmax(mx)) * C2;
  float l = 0.f;
  constexpr int NV = NS * 4 * U / 256;
#pragma unroll
  for (int i = 0; i < NV; ++i) {
    f32x4 t = *reinterpret_cast<const f32x4*>(pl + (i * 64 + lane) * 4);
#pragma unroll
    for (int j = 0; j < 4; ++j) { t[j] = __builtin_amdgcn_exp2f(fmaf(t[j], C2, -mw)); l += t[j]; }
    *reinterpret_cast<f32x4*>(pl + (i * 64 + lane) * 4) = t;
  }
  if (lane < NG * 4 * U) pl[NS * 4 * U + lane] = 0.f;
  asm volatile("" ::: "memory");
  l = xrow_sum(row_allsum(l));
  float acc[8];
#pragma unroll
  for (int i = 0; i < 8; ++i) acc[i] = 0.f;
  const float* pr = pl + r * U;
  float pn[U];
#pragma unroll
  for (int u = 0; u < U; ++u) pn[u] = pr[u];
  for (int j0 = 0; j0 < NS; j0 += NG) {
#pragma unroll
    for (int jj = 0; jj < NG; ++jj) {
      const int g = (NS % NG + jj) % NG;
      const int so = wo + (j0 + jj + NG) * GB;
      float p[U];
#pragma unroll
      for (int u = 0; u < U; ++u) p[u] = pn[u];
#pragma unroll
      for (int u = 0; u < U; ++u) pn[u] = pr[(j0 + jj + 1 < NS + NG - 1 ? j0 + jj + 1 : 0) * 4 * U + u];
#pragma unroll
      for (int u = 0; u < U; ++u) {
#pragma unroll
        for (int j = 0; j < 4; ++j) {
          const unsigned w = R[g][u][j];
          acc[2 * j] = fmaf(p[u], __builtin_bit_cast(float, w << 16), acc[2 * j]);
          acc[2 * j + 1] = fmaf(p[u], __builtin_bit_cast(float, w & 0xffff0000u), acc[2 * j + 1]);
        }
      }
      asm volatile("" : "+v"(acc[0]), "+v"(acc[1]), "+v"(acc[2]), "+v"(acc[3]), "+v"(acc[4]), "+v"(acc[5]), "+v"(acc[6]), "+v"(acc[7]));
      SB();
      if (j0 + jj + NG < NS) pace(NS + j0 + jj + NG);
#pragma unroll
      for (int u = 0; u < U; ++u) R[g][u] = __builtin_amdgcn_raw_buffer_load_b128(rv, voff + u * 1024, so, AUX);
      SB();
    }
  }
#pragma unroll
  for (int i = 0; i < 8; ++i) acc[i] = xrow_sum(acc[i]);
  write_record(lds + wave * 132, mw, l, acc, r, c);
}

template <int NW, int U, int NGA, int NGB, int NA, int MAP, int PACE, bool STMP>
__global__ void __launch_bounds__((NW + 1) * 64) attn_k(const uint16_t* __restrict__ q, const uint16_t* __restrict__ k, const uint16_t* __restrict__ v,
                                                        __hip_bfloat16* __restrict__ out, unsigned char* __restrict__ ws) {
  constexpr int NGM = NGA > NGB ? NGA : NGB;
  __shared__ __attribute__((aligned(16))) float lds[NW * 132 + 2048 + NW * NGM * 4 * U];
  const unsigned long long tstart = STMP ? __builtin_amdgcn_s_memrealtime() : 0ull;
  const int bid = map_bid<MAP>(blockIdx.x);
  const int tid = threadIdx.x, lane = tid & 63, wave = __builtin_amdgcn_readfirstlane(tid >> 6);
  if (__builtin_expect(wave == NW, 0)) { protocol<NW, STMP>(out, ws, lds, bid, lane, tstart); return; }
  if (NGA == NGB || wave < NA) stream2<NW, U, NGA, NGM, PACE>(q, k, v, lds, bid, lane, wave);
  else stream2<NW, U, NGB, NGM, PACE>(q, k, v, lds, bid, lane, wave);
  __syncthreads();
}

template <int NW, int U, int NG, int MAP, int PV, bool STMP>
__global__ void __launch_bounds__((NW + 1) * 64) attn_null(const uint16_t* __restrict__ q, const uint16_t* __restrict__ k, const uint16_t* __restrict__ v,
                                                           __hip_bfloat16* __restrict__ out, unsigned char* __restrict__ ws) {
  __shared__ __attribute__((aligned(16))) float lds[NW * 132];
  const unsigned long long tstart = STMP ? __builtin_amdgcn_s_memrealtime() : 0ull;
  const int bid = map_bid<MAP>(blockIdx.x);
  const int tid = threadIdx.x, lane = tid & 63, wave = __builtin_amdgcn_readfirstlane(tid >> 6);
  if (__builtin_expect(wave == NW, 0)) { protocol<NW, STMP, PV>(out, ws, lds, bid, lane, tstart); return; }
  constexpr int NS = 2048 / (NW * 4 * U), GB = NW * U * 1024;
  auto rk = __builtin_amdgcn_make_buffer_rsrc((void*)(k + (size_t)bid * 2048 * D), 0, 2048 * D * 2, 0x00020000);
  auto rv = __builtin_amdgcn_make_buffer_rsrc((void*)(v + (size_t)bid * 2048 * D), 0, 2048 * D * 2, 0x00020000);
  const int voff = lane * 16, wo = wave * U * 1024;
  u32x4 R[NG][U];
#pragma unroll
  for (int g = 0; g < NG; ++g) {
#pragma unroll
    for (int u = 0; u < U; ++u) R[g][u] = __builtin_amdgcn_raw_buffer_load_b128(rk, voff + u * 1024, wo + g * GB, AUX);
    SB();
  }
  u32x4 acc = {0, 0, 0, 0};
  for (int s0 = 0; s0 < 2 * NS; s0 += NG) {
#pragma unroll
    for (int g = 0; g < NG; ++g) {
      const int n = s0 + g + NG;
      const bool isv = n >= NS;
      const int so = wo + (isv ? n - NS : n) * GB;
#pragma unroll
      for (int u = 0; u < U; ++u) acc ^= R[g][u];
      asm volatile("" : "+v"(acc));
      SB();
#pragma unroll
      for (int u = 0; u < U; ++u) R[g][u] = __builtin_amdgcn_raw_buffer_load_b128(isv ? rv : rk, voff + u * 1024, so, AUX);
      SB();
    }
  }
  float a8[8] = {1.f, 1.f, 1.f, 1.f, 1.f, 1.f, 1.f, __uint_as_float((acc[0] ^ acc[1] ^ acc[2] ^ acc[3]) & 0x3fffffffu)};
  write_record(lds + wave * 132, 0.f, 1.f, a8, lane >> 4, lane & 15);
  __syncthreads();
}

template <int NW, int U, int NG, int MAP, int ROT, bool STMP>
__global__ void __launch_bounds__((NW + 1) * 64) attn_null2(const uint16_t* __restrict__ q, const uint16_t* __restrict__ k, const uint16_t* __restrict__ v,
                                                            __hip_bfloat16* __restrict__ out, unsigned char* __restrict__ ws) {
  const unsigned long long tstart = __builtin_amdgcn_s_memrealtime();
  const int bid = map_bid<MAP>(blockIdx.x);
  const int tid = threadIdx.x, lane = tid & 63, wave = __builtin_amdgcn_readfirstlane(tid >> 6);
  if (wave == NW) { __syncthreads(); return; }
  constexpr int NS = 2048 / (NW * 4 * U), GB = NW * U * 1024;
  auto rk = __builtin_amdgcn_make_buffer_rsrc((void*)(k + (size_t)bid * 2048 * D), 0, 2048 * D * 2, 0x00020000);
  auto rv = __builtin_amdgcn_make_buffer_rsrc((void*)(v + (size_t)bid * 2048 * D), 0, 2048 * D * 2, 0x00020000);
  const int voff = lane * 16, wo = wave * U * 1024;
  const int rot = ROT == 0 ? 0 : ROT == 1 ? (bid * (NS / 32)) % NS : ROT == 2 ? (int)((__builtin_bitreverse32((unsigned)bid) >> 24) * NS / 256) : ROT == 3 ? (int)(((unsigned)bid * 2654435761u >> 16) % NS) : ((bid >> 3) * (NS / 32)) % NS;
  u32x4 R[NG][U];
#pragma unroll
  for (int g = 0; g < NG; ++g) {
#pragma unroll
    for (int u = 0; u < U; ++u) R[g][u] = __builtin_amdgcn_raw_buffer_load_b128(rk, voff + u * 1024, wo + ((g + rot) % NS) * GB, AUX);
    SB();
  }
  u32x4 acc = {0, 0, 0, 0};
  for (int s0 = 0; s0 < 2 * NS; s0 += NG) {
#pragma unroll
    for (int g = 0; g < NG; ++g) {
      const int n = s0 + g + NG;
      const bool isv = n >= NS;
      const int so = n >= 2 * NS ? OOB : wo + ((n + rot) % NS) * GB;
#pragma unroll
      for (int u = 0; u < U; ++u) acc ^= R[g][u];
      asm volatile("" : "+v"(acc));
      SB();
#pragma unroll
      for (int u = 0; u < U; ++u) R[g][u] = __builtin_amdgcn_raw_buffer_load_b128(isv ? rv : rk, voff + u * 1024, so, AUX);
      SB();
    }
  }
  if ((acc[0] ^ acc[1] ^ acc[2] ^ acc[3]) == 0x12345u) out[tid] = __float2bfloat16(1.f);
  __syncthreads();
  if (tid == 0) {
    unsigned long long* st = (unsigned long long*)(ws + WS_STAMP) + (size_t)blockIdx.x * 4;
    unsigned xcc; asm volatile("s_getreg_b32 %0, hwreg(HW_REG_XCC_ID)" : "=s"(xcc));
    const unsigned long long t = __builtin_amdgcn_s_memrealtime();
    st[0] = tstart; st[1] = t; st[2] = t; st[3] = (xcc & 15);
  }
}

#define STAMPS 0
extern "C" void kernel_launch(void* const* d_in, const int* in_sizes, int n_in, void* d_out, int out_size, void* d_ws,
                              size_t ws_size, hipStream_t stream) {
  attn_k<4, 4, 2, 2, 0, 3, 3500, STAMPS != 0><<<NWG, (4 + 1) * 64, 0, stream>>>((const uint16_t*)d_in[0], (const uint16_t*)d_in[1], (const uint16_t*)d_in[2],
                                                    (__hip_bfloat16*)d_out, (unsigned char*)d_ws);
}
```

```cpp
#include <hip/hip_runtime.h>
#include <hip/hip_bf16.h>
#include <stdint.h>

constexpr int BH = 128, SKV = 4096, D = 128;
constexpr int NWG = BH * 2;
constexpr unsigned TOK_ENTRY = 0x454e5452u, TOK_READY = 0x52454459u, CLEARED = 0xffffffffu;
constexpr size_t WS_MBOX = (size_t)NWG * 1024;
constexpr size_t WS_STAMP = 1 << 20;
#define AUX 2

typedef unsigned u32x4 __attribute__((ext_vector_type(4)));
typedef __bf16 bf16x2 __attribute__((ext_vector_type(2)));
typedef float f32x4 __attribute__((ext_vector_type(4)));
typedef float f32x2 __attribute__((ext_vector_type(2)));
#define GAS __attribute__((address_space(1)))
#define LAS __attribute__((address_space(3)))
typedef GAS unsigned gu32;
typedef GAS unsigned long long gu64;
#define RLX_AGENT __ATOMIC_RELAXED, __HIP_MEMORY_SCOPE_AGENT

__device__ __forceinline__ float dot2(unsigned a, unsigned b, float acc) {
  return __builtin_amdgcn_fdot2_f32_bf16(__builtin_bit_cast(bf16x2, a), __builtin_bit_cast(bf16x2, b), acc, false);
}
template <int CTRL> __device__ __forceinline__ float dpp(float x) {
  return __builtin_bit_cast(float, __builtin_amdgcn_mov_dpp(__builtin_bit_cast(int, x), CTRL, 0xf, 0xf, true));
}
__device__ __forceinline__ float row_allsum(float x) {
  x += dpp<0x128>(x); x += dpp<0x124>(x); x += dpp<0x122>(x); x += dpp<0x121>(x);
  return x;
}
__device__ __forceinline__ float row_allmax(float x) {
  x = fmaxf(x, dpp<0x128>(x)); x = fmaxf(x, dpp<0x124>(x)); x = fmaxf(x, dpp<0x122>(x)); x = fmaxf(x, dpp<0x121>(x));
  return x;
}
__device__ __forceinline__ float xrow_max(float x) {
  auto s = __builtin_amdgcn_permlane16_swap(__float_as_uint(x), __float_as_uint(x), false, false);
  x = fmaxf(__uint_as_float(s[0]), __uint_as_float(s[1]));
  auto t = __builtin_amdgcn_permlane32_swap(__float_as_uint(x), __float_as_uint(x), false, false);
  return fmaxf(__uint_as_float(t[0]), __uint_as_float(t[1]));
}
__device__ __forceinline__ float xrow_sum(float x) {
  auto s = __builtin_amdgcn_permlane16_swap(__float_as_uint(x), __float_as_uint(x), false, false);
  x = __uint_as_float(s[0]) + __uint_as_float(s[1]);
  auto t = __builtin_amdgcn_permlane32_swap(__float_as_uint(x), __float_as_uint(x), false, false);
  return __uint_as_float(t[0]) + __uint_as_float(t[1]);
}
__device__ __forceinline__ unsigned now32() { return (unsigned)__builtin_amdgcn_s_memrealtime(); }
#define SB() __builtin_amdgcn_sched_barrier(0)
#define DRAIN() asm volatile("s_waitcnt vmcnt(0)" ::: "memory")
constexpr unsigned TMO = 20000000u;
constexpr float C2 = 0.088388347648318440f * 1.4426950408889634f;

template <int NW, int U, int NG, int PACE, int MODE, bool STMP>
__global__ void __launch_bounds__((NW + 1) * 64) attn_p(const uint16_t* __restrict__ q, const uint16_t* __restrict__ k, const uint16_t* __restrict__ v,
                                                        __hip_bfloat16* __restrict__ out, unsigned char* __restrict__ ws) {
  constexpr int NS = 2048 / (NW * 4 * U);
  constexpr int KM = ((NS - NG) / NG) * NG;
  constexpr int PW = (NS + NG) * 4 * U;
  constexpr int GB = NW * U * 1024;
  constexpr int L_REC = 4, L_P = L_REC + NW * 132;
  __shared__ __attribute__((aligned(16))) float lds[L_P + NW * PW];
  const unsigned long long tstart = __builtin_amdgcn_s_memrealtime();
  const int blk = blockIdx.x, x = blk & 7;
  const int bh = (x & 3) * 32 + (blk >> 3), sp = x >> 2;
  const int tid = threadIdx.x, lane = tid & 63, wave = __builtin_amdgcn_readfirstlane(tid >> 6);
  volatile LAS unsigned long long* ctl = (volatile LAS unsigned long long*)lds;
  if (__builtin_expect(wave == NW, 0)) {
    if (lane == 0) *ctl = 0ull;
    __syncthreads();
    gu64* inbox_me = (gu64*)(ws + (size_t)blk * 1024) + lane;
    gu64* inbox_pt = (gu64*)(ws + (size_t)(blk ^ 4) * 1024) + lane;
    const int j = lane & 7;
    const bool act = lane < 8 && j != x;
    gu64* mb_own = (gu64*)(ws + WS_MBOX + (size_t)blk * 128) + j;
    gu64* mb_peer = (gu64*)(ws + WS_MBOX + (size_t)((blk & ~7) + j) * 128) + x;
    const unsigned s_me = (unsigned)tstart;
    const unsigned long long ready = ((unsigned long long)TOK_READY << 32) | s_me;
    __hip_atomic_store(inbox_me, ~0ull, RLX_AGENT);
    __hip_atomic_store(inbox_me + 64, ~0ull, RLX_AGENT);
    if (act) __hip_atomic_store(mb_own, (unsigned long long)TOK_ENTRY << 32, RLX_AGENT);
    DRAIN();
    if (act) __hip_atomic_store(mb_peer, ready, RLX_AGENT);
    unsigned warm = ((const GAS unsigned*)out)[bh * (D / 2) + lane];
    asm volatile("" :: "v"(warm));
    DRAIN();
    for (bool got = false;;) {
      unsigned long long own = ready, par = ready;
      if (act) { own = __hip_atomic_load(mb_own, RLX_AGENT); par = __hip_atomic_load(mb_peer, RLX_AGENT); }
      const bool rs = (unsigned)(par >> 32) == TOK_ENTRY;
      if (__any(rs)) {
        if (rs) __hip_atomic_store(mb_peer, ready, RLX_AGENT);
        DRAIN();
      }
      if (got) break;
      if (__all((unsigned)(own >> 32) == TOK_READY)) {
        got = true;
        int d = (int)((unsigned)own - s_me);
        if (MODE == 1) { d += __shfl_xor(d, 1); d += __shfl_xor(d, 2); d += __shfl_xor(d, 4); d >>= 3; }
        if (MODE == 2) { d = min(d, __shfl_xor(d, 1)); d = min(d, __shfl_xor(d, 2)); d = min(d, __shfl_xor(d, 4)); }
        if (MODE == 3) { d = max(d, __shfl_xor(d, 1)); d = max(d, __shfl_xor(d, 2)); d = max(d, __shfl_xor(d, 4)); }
        if (lane == 0) *ctl = (1ull << 32) | (unsigned)(s_me + d + PACE);
        continue;
      }
      if (now32() - s_me > TMO) break;
      __builtin_amdgcn_s_sleep(24);
    }
    __syncthreads();
    const unsigned long long tstream = STMP ? __builtin_amdgcn_s_memrealtime() : 0ull;
    unsigned long long x0 = __hip_atomic_load(inbox_me, RLX_AGENT), x1 = __hip_atomic_load(inbox_me + 64, RLX_AGENT);
    const float* rec = lds + L_REC;
    float M = -INFINITY;
    f32x2 ml[NW];
#pragma unroll
    for (int w = 0; w < NW; ++w) { ml[w] = *reinterpret_cast<const f32x2*>(rec + w * 132); M = fmaxf(M, ml[w][0]); }
    float L = 0.f, o0 = 0.f, o1 = 0.f;
#pragma unroll
    for (int w = 0; w < NW; ++w) {
      const float wt = __builtin_amdgcn_exp2f(ml[w][0] - M);
      L = fmaf(wt, ml[w][1], L);
      o0 = fmaf(wt, rec[w * 132 + 4 + lane], o0);
      o1 = fmaf(wt, rec[w * 132 + 68 + lane], o1);
    }
    const float inv = 1.0f / L;
    o0 *= inv; o1 *= inv;
    const float lse = M + __builtin_amdgcn_logf(L);
    unsigned tag = __float_as_uint(lse);
    if (tag == CLEARED) tag = 0x7fc00000u;
    __hip_atomic_store(inbox_pt, ((unsigned long long)tag << 32) | __float_as_uint(o0), RLX_AGENT);
    __hip_atomic_store(inbox_pt + 64, ((unsigned long long)tag << 32) | __float_as_uint(o1), RLX_AGENT);
    SB();
    asm volatile("" : "+v"(x0), "+v"(x1));
    bool have = __all((unsigned)(x0 >> 32) != CLEARED && (unsigned)(x1 >> 32) != CLEARED);
    if (!have) {
      DRAIN();
      x0 = __hip_atomic_load(inbox_me, RLX_AGENT); x1 = __hip_atomic_load(inbox_me + 64, RLX_AGENT);
      have = __all((unsigned)(x0 >> 32) != CLEARED && (unsigned)(x1 >> 32) != CLEARED);
    }
    if (have) {
      const float lse_me = __uint_as_float(tag), lse_pt = __uint_as_float((unsigned)(x0 >> 32));
      const float lseA = sp == 0 ? lse_me : lse_pt, lseB = sp == 0 ? lse_pt : lse_me;
      const float p0 = __uint_as_float((unsigned)x0), p1 = __uint_as_float((unsigned)x1);
      const float a0 = sp == 0 ? o0 : p0, b0 = sp == 0 ? p0 : o0, a1 = sp == 0 ? o1 : p1, b1 = sp == 0 ? p1 : o1;
      const float MM = fmaxf(lseA, lseB);
      const float wA = __builtin_amdgcn_exp2f(lseA - MM), wB = __builtin_amdgcn_exp2f(lseB - MM);
      const float iw = 1.0f / (wA + wB);
      out[bh * D + lane] = __float2bfloat16(fmaf(wA, a0, wB * b0) * iw);
      out[bh * D + 64 + lane] = __float2bfloat16(fmaf(wA, a1, wB * b1) * iw);
    }
    if (STMP && lane == 0) {
      DRAIN();
      unsigned long long* st = (unsigned long long*)(ws + WS_STAMP) + (size_t)blockIdx.x * 4;
      unsigned xcc; asm volatile("s_getreg_b32 %0, hwreg(HW_REG_XCC_ID)" : "=s"(xcc));
      st[0] = tstart; st[1] = tstream; st[2] = __builtin_amdgcn_s_memrealtime(); st[3] = (xcc & 15) | ((unsigned long long)have << 8);
    }
    return;
  }
  const int r = lane >> 4, c = lane & 15;
  const size_t eo = ((size_t)bh * 2 + sp) * 2048 * D;
  auto rk = __builtin_amdgcn_make_buffer_rsrc((void*)(k + eo), 0, 2048 * D * 2, 0x00020000);
  auto rv = __builtin_amdgcn_make_buffer_rsrc((void*)(v + eo), 0, 2048 * D * 2, 0x00020000);
  const int voff = lane * 16;
  const int wo = wave * U * 1024;
  const u32x4 qv = *reinterpret_cast<const u32x4*>(q + (size_t)bh * D + 8 * c);
  SB();
  u32x4 R[NG][U];
#pragma unroll
  for (int g = 0; g < NG; ++g) {
#pragma unroll
    for (int u = 0; u < U; ++u) R[g][u] = __builtin_amdgcn_raw_buffer_load_b128(rk, voff + u * 1024, wo + g * GB, AUX);
    SB();
  }
  __syncthreads();
  unsigned pt0 = (unsigned)tstart, ptau = MODE == 0 ? (unsigned)(PACE * 256 / (2 * NS)) : 0u;
  int pn0 = 0;
  bool pseen = MODE == 0 || PACE == 0;
  auto pace = [&](int n) {
    if (PACE == 0) return;
    if (!pseen) {
      const unsigned long long cv = *ctl;
      if (__builtin_amdgcn_readfirstlane((unsigned)(cv >> 32)) == 0u) return;
      pseen = true;
      pt0 = now32(); pn0 = n;
      const int left = (int)(__builtin_amdgcn_readfirstlane((unsigned)cv) - pt0);
      ptau = left > 0 ? __builtin_amdgcn_readfirstlane((unsigned)((float)left * 256.0f / (float)(2 * NS - n))) : 0u;
      return;
    }
    const unsigned due = ((unsigned)(n - pn0) * ptau) >> 8;
    while (now32() - pt0 < due) __builtin_amdgcn_s_sleep(1);
  };
  float* pl = lds + L_P + wave * PW;
  float* pw = pl + r * U + (c & (U - 1));
  float mx = -INFINITY;
  auto kstep = [&](int g, int s, __amdgpu_buffer_rsrc_t rn, int so) {
    float sc[U];
#pragma unroll
    for (int u = 0; u < U; ++u) {
      float d = dot2(R[g][u][0], qv[0], 0.f);
      d = dot2(R[g][u][1], qv[1], d); d = dot2(R[g][u][2], qv[2], d); d = dot2(R[g][u][3], qv[3], d);
      sc[u] = d;
      asm volatile("" : "+v"(sc[u]));
    }
    SB();
    pace(s + NG);
#pragma unroll
    for (int u = 0; u < U; ++u) R[g][u] = __builtin_amdgcn_raw_buffer_load_b128(rn, voff + u * 1024, so, AUX);
    SB();
    float t = row_allsum(sc[0]);
#pragma unroll
    for (int u = 1; u < U; ++u) { const float tu = row_allsum(sc[u]); t = (c & (U - 1)) == u ? tu : t; }
    mx = fmaxf(mx, t);
    if (c < U) pw[s * 4 * U] = t;
    asm volatile("" : "+v"(mx));
  };
  for (int s0 = 0; s0 < KM; s0 += NG) {
#pragma unroll
    for (int g = 0; g < NG; ++g) kstep(g, s0 + g, rk, wo + (s0 + g + NG) * GB);
  }
#pragma unroll
  for (int s = KM; s < NS; ++s) {
    if (s + NG < NS) kstep((s - KM) % NG, s, rk, wo + (s + NG) * GB);
    else kstep((s - KM) % NG, s, rv, wo + (s + NG - NS) * GB);
  }
  asm volatile("" ::: "memory");
  const float mw = xrow_max(row_allmax(mx)) * C2;
  float l = 0.f;
  constexpr int NV = NS * 4 * U / 256;
#pragma unroll
  for (int i = 0; i < NV; ++i) {
    f32x4 t = *reinterpret_cast<const f32x4*>(pl + (i * 64 + lane) * 4);
#pragma unroll
    for (int jx = 0; jx < 4; ++jx) { t[jx] = __builtin_amdgcn_exp2f(fmaf(t[jx], C2, -mw)); l += t[jx]; }
    *reinterpret_cast<f32x4*>(pl + (i * 64 + lane) * 4) = t;
  }
  for (int i = lane; i < NG * 4 * U; i += 64) pl[NS * 4 * U + i] = 0.f;
  asm volatile("" ::: "memory");
  l = xrow_sum(row_allsum(l));
  float acc[8];
#pragma unroll
  for (int i = 0; i < 8; ++i) acc[i] = 0.f;
  const float* pr = pl + r * U;
  float pn[U];
#pragma unroll
  for (int u = 0; u < U; ++u) pn[u] = pr[u];
  for (int j0 = 0; j0 < NS; j0 += NG) {
#pragma unroll
    for (int jj = 0; jj < NG; ++jj) {
      const int g = (NS % NG + jj) % NG;
      const int so = wo + (j0 + jj + NG) * GB;
      float p[U];
#pragma unroll
      for (int u = 0; u < U; ++u) p[u] = pn[u];
#pragma unroll
      for (int u = 0; u < U; ++u) pn[u] = pr[(j0 + jj + 1) * 4 * U + u];
#pragma unroll
      for (int u = 0; u < U; ++u) {
#pragma unroll
        for (int jx = 0; jx < 4; ++jx) {
          const unsigned w = R[g][u][jx];
          acc[2 * jx] = fmaf(p[u], __builtin_bit_cast(float, w << 16), acc[2 * jx]);
          acc[2 * jx + 1] = fmaf(p[u], __builtin_bit_cast(float, w & 0xffff0000u), acc[2 * jx + 1]);
        }
      }
      asm volatile("" : "+v"(acc[0]), "+v"(acc[1]), "+v"(acc[2]), "+v"(acc[3]), "+v"(acc[4]), "+v"(acc[5]), "+v"(acc[6]), "+v"(acc[7]));
      SB();
      if (j0 + jj + NG < NS) pace(NS + j0 + jj + NG);
#pragma unroll
      for (int u = 0; u < U; ++u) R[g][u] = __builtin_amdgcn_raw_buffer_load_b128(rv, voff + u * 1024, so, AUX);
      SB();
    }
  }
#pragma unroll
  for (int i = 0; i < 8; ++i) acc[i] = xrow_sum(acc[i]);
  float* rec = lds + L_REC + wave * 132;
  if (r == 0) {
    *reinterpret_cast<f32x4*>(rec + 4 + 8 * c) = f32x4{acc[0], acc[1], acc[2], acc[3]};
    *reinterpret_cast<f32x4*>(rec + 8 + 8 * c) = f32x4{acc[4], acc[5], acc[6], acc[7]};
    if (c == 0) *reinterpret_cast<f32x2*>(rec) = f32x2{mw, l};
  }
  __syncthreads();
}

#define STAMPS 0
extern "C" void kernel_launch(void* const* d_in, const int* in_sizes, int n_in, void* d_out, int out_size, void* d_ws,
                              size_t ws_size, hipStream_t stream) {
  attn_p<4,4,2,3500,1, STAMPS != 0><<<NWG, (4 + 1) * 64, 0, stream>>>((const uint16_t*)d_in[0], (const uint16_t*)d_in[1], (const uint16_t*)d_in[2],
                                                    (__hip_bfloat16*)d_out, (unsigned char*)d_ws);
}
```

```cpp
#include <hip/hip_runtime.h>
#include <hip/hip_bf16.h>
#include <stdint.h>

constexpr int BH = 128, SKV = 4096, D = 128;
constexpr int NWG = BH * 2;
constexpr unsigned TOK_ENTRY = 0x454e5452u, TOK_READY = 0x52454459u, CLEARED = 0xffffffffu;
constexpr int MB_STRIDE = 4352;
constexpr size_t WS_MBOX = (size_t)NWG * 1024;
constexpr size_t WS_STAMP = 4 << 20;
#define AUX_NT 2
#define AUX_SC1 16

typedef unsigned u32x4 __attribute__((ext_vector_type(4)));
typedef __bf16 bf16x2 __attribute__((ext_vector_type(2)));
typedef float f32x4 __attribute__((ext_vector_type(4)));
typedef float f32x2 __attribute__((ext_vector_type(2)));
#define GAS __attribute__((address_space(1)))
#define LAS __attribute__((address_space(3)))
typedef GAS unsigned long long gu64;
#define RLX_AGENT __ATOMIC_RELAXED, __HIP_MEMORY_SCOPE_AGENT

__device__ __forceinline__ float dot2(unsigned a, unsigned b, float acc) {
  return __builtin_amdgcn_fdot2_f32_bf16(__builtin_bit_cast(bf16x2, a), __builtin_bit_cast(bf16x2, b), acc, false);
}
template <int CTRL> __device__ __forceinline__ float dpp(float x) {
  return __builtin_bit_cast(float, __builtin_amdgcn_mov_dpp(__builtin_bit_cast(int, x), CTRL, 0xf, 0xf, true));
}
__device__ __forceinline__ float row_allsum(float x) {
  x += dpp<0x128>(x); x += dpp<0x124>(x); x += dpp<0x122>(x); x += dpp<0x121>(x);
  return x;
}
__device__ __forceinline__ float row_allmax(float x) {
  x = fmaxf(x, dpp<0x128>(x)); x = fmaxf(x, dpp<0x124>(x)); x = fmaxf(x, dpp<0x122>(x)); x = fmaxf(x, dpp<0x121>(x));
  return x;
}
__device__ __forceinline__ float xrow_max(float x) {
  auto s = __builtin_amdgcn_permlane16_swap(__float_as_uint(x), __float_as_uint(x), false, false);
  x = fmaxf(__uint_as_float(s[0]), __uint_as_float(s[1]));
  auto t = __builtin_amdgcn_permlane32_swap(__float_as_uint(x), __float_as_uint(x), false, false);
  return fmaxf(__uint_as_float(t[0]), __uint_as_float(t[1]));
}
__device__ __forceinline__ float xrow_sum(float x) {
  auto s = __builtin_amdgcn_permlane16_swap(__float_as_uint(x), __float_as_uint(x), false, false);
  x = __uint_as_float(s[0]) + __uint_as_float(s[1]);
  auto t = __builtin_amdgcn_permlane32_swap(__float_as_uint(x), __float_as_uint(x), false, false);
  return __uint_as_float(t[0]) + __uint_as_float(t[1]);
}
__device__ __forceinline__ unsigned now32() { return (unsigned)__builtin_amdgcn_s_memrealtime(); }
#define SB() __builtin_amdgcn_sched_barrier(0)
#define DRAIN() asm volatile("s_waitcnt vmcnt(0)" ::: "memory")
constexpr unsigned TMO = 20000000u;
constexpr float C2 = 0.088388347648318440f * 1.4426950408889634f;

template <int NW, int U, int NG, int PACE, int STAG, bool STMP>
__global__ void __launch_bounds__((NW + 1) * 64) attn_p(const uint16_t* __restrict__ q, const uint16_t* __restrict__ k, const uint16_t* __restrict__ v,
                                                        __hip_bfloat16* __restrict__ out, unsigned char* __restrict__ ws) {
  constexpr int NS = 2048 / (NW * 4 * U);
  constexpr int KM = ((NS - NG) / NG) * NG;
  constexpr int PW = (NS + NG) * 4 * U;
  constexpr int GB = NW * U * 1024;
  constexpr int L_FLAG = 4, L_REC = 4 + 16, L_P = L_REC + NW * 132;
  __shared__ __attribute__((aligned(16))) float lds[L_P + NW * PW];
  const unsigned long long tstart = __builtin_amdgcn_s_memrealtime();
  const int blk = blockIdx.x, x = blk & 7;
  const int bh = (x & 3) * 32 + (blk >> 3), sp = x >> 2;
  const int tid = threadIdx.x, lane = tid & 63, wave = __builtin_amdgcn_readfirstlane(tid >> 6);
  volatile LAS unsigned long long* ctl = (volatile LAS unsigned long long*)lds;
  volatile LAS unsigned* flag = (volatile LAS unsigned*)(lds + L_FLAG);
  if (__builtin_expect(wave == NW, 0)) {
    if (lane == 0) *ctl = 0ull;
    if (lane < NW) flag[lane] = 0u;
    __syncthreads();
    auto rin = __builtin_amdgcn_make_buffer_rsrc((void*)ws, 0, (int)WS_MBOX, 0x00020000);
    const int in_me = blk * 1024, in_pt = (blk ^ 4) * 1024, lo = lane * 16;
    gu64* mb_me = (gu64*)(ws + WS_MBOX + (size_t)blk * MB_STRIDE);
    gu64* mb_pt = (gu64*)(ws + WS_MBOX + (size_t)(blk ^ 4) * MB_STRIDE);
    const unsigned s_me = (unsigned)tstart;
    const unsigned long long ready = ((unsigned long long)TOK_READY << 32) | s_me;
    __builtin_amdgcn_raw_buffer_store_b128(u32x4{CLEARED, CLEARED, CLEARED, CLEARED}, rin, lo, in_me, AUX_SC1);
    if (lane == 0) __hip_atomic_store(mb_me, (unsigned long long)TOK_ENTRY << 32, RLX_AGENT);
    DRAIN();
    if (lane == 0) __hip_atomic_store(mb_pt, ready, RLX_AGENT);
    unsigned warm = ((const GAS unsigned*)out)[bh * (D / 2) + lane];
    asm volatile("" :: "v"(warm));
    DRAIN();
    for (bool got = false;;) {
      const unsigned long long own = __hip_atomic_load(mb_me, RLX_AGENT), par = __hip_atomic_load(mb_pt, RLX_AGENT);
      const unsigned own_t = __builtin_amdgcn_readfirstlane((unsigned)(own >> 32)), own_s = __builtin_amdgcn_readfirstlane((unsigned)own);
      if (__builtin_amdgcn_readfirstlane((unsigned)(par >> 32)) == TOK_ENTRY) {
        if (lane == 0) __hip_atomic_store(mb_pt, ready, RLX_AGENT);
        DRAIN();
      }
      if (got) break;
      if (own_t == TOK_READY) {
        got = true;
        if (lane == 0) *ctl = (1ull << 32) | (unsigned)(s_me + ((int)(own_s - s_me) >> 1) + PACE + (sp ? -STAG : STAG));
        continue;
      }
      if (now32() - s_me > TMO) break;
      __builtin_amdgcn_s_sleep(16);
    }
    u32x4 xp = {CLEARED, CLEARED, CLEARED, CLEARED};
    bool have = false;
    for (unsigned tp = s_me;;) {
      const unsigned f = lane < NW ? flag[lane] : 2u;
      if (__all(f == 2u)) break;
      if (__any(f == 0u)) { __builtin_amdgcn_s_sleep(32); continue; }
      const unsigned t = now32();
      if (!have && t - tp >= 40u) {
        tp = t;
        xp = __builtin_amdgcn_raw_buffer_load_b128(rin, lo, in_me, AUX_SC1);
        have = __all(xp[1] != CLEARED && xp[3] != CLEARED);
      } else __builtin_amdgcn_s_sleep(1);
      if (t - s_me > TMO) break;
    }
    asm volatile("" ::: "memory");
    const unsigned long long tstream = STMP ? __builtin_amdgcn_s_memrealtime() : 0ull;
    if (!have) xp = __builtin_amdgcn_raw_buffer_load_b128(rin, lo, in_me, AUX_SC1);
    const float* rec = lds + L_REC;
    float M = -INFINITY;
    f32x2 ml[NW];
#pragma unroll
    for (int w = 0; w < NW; ++w) { ml[w] = *reinterpret_cast<const f32x2*>(rec + w * 132); M = fmaxf(M, ml[w][0]); }
    float L = 0.f, o0 = 0.f, o1 = 0.f;
#pragma unroll
    for (int w = 0; w < NW; ++w) {
      const float wt = __builtin_amdgcn_exp2f(ml[w][0] - M);
      const f32x2 a = *reinterpret_cast<const f32x2*>(rec + w * 132 + 4 + 2 * lane);
      L = fmaf(wt, ml[w][1], L);
      o0 = fmaf(wt, a[0], o0);
      o1 = fmaf(wt, a[1], o1);
    }
    const float inv = 1.0f / L;
    o0 *= inv; o1 *= inv;
    unsigned tag = __float_as_uint(M + __builtin_amdgcn_logf(L));
    if (tag == CLEARED) tag = 0x7fc00000u;
    if (!have) {
      __builtin_amdgcn_raw_buffer_store_b128(u32x4{__float_as_uint(o0), tag, __float_as_uint(o1), tag}, rin, lo, in_pt, AUX_SC1);
      SB();
      asm volatile("" : "+v"(xp));
      have = __all(xp[1] != CLEARED && xp[3] != CLEARED);
      if (!have) {
        DRAIN();
        xp = __builtin_amdgcn_raw_buffer_load_b128(rin, lo, in_me, AUX_SC1);
        have = __all(xp[1] != CLEARED && xp[3] != CLEARED);
      }
    }
    if (have) {
      const float lse_me = __uint_as_float(tag), lse_pt = __uint_as_float(xp[1]);
      const float lseA = sp == 0 ? lse_me : lse_pt, lseB = sp == 0 ? lse_pt : lse_me;
      const float p0 = __uint_as_float(xp[0]), p1 = __uint_as_float(xp[2]);
      const float a0 = sp == 0 ? o0 : p0, b0 = sp == 0 ? p0 : o0, a1 = sp == 0 ? o1 : p1, b1 = sp == 0 ? p1 : o1;
      const float MM = fmaxf(lseA, lseB);
      const float wA = __builtin_amdgcn_exp2f(lseA - MM), wB = __builtin_amdgcn_exp2f(lseB - MM);
      const float iw = 1.0f / (wA + wB);
      const __hip_bfloat16 r0 = __float2bfloat16(fmaf(wA, a0, wB * b0) * iw), r1 = __float2bfloat16(fmaf(wA, a1, wB * b1) * iw);
      reinterpret_cast<unsigned*>(out)[bh * (D / 2) + lane] =
          (unsigned)__builtin_bit_cast(unsigned short, r0) | ((unsigned)__builtin_bit_cast(unsigned short, r1) << 16);
    }
    if (STMP && lane == 0) {
      DRAIN();
      unsigned long long* st = (unsigned long long*)(ws + WS_STAMP) + (size_t)blockIdx.x * 4;
      unsigned xcc; asm volatile("s_getreg_b32 %0, hwreg(HW_REG_XCC_ID)" : "=s"(xcc));
      st[0] = tstart; st[1] = tstream; st[2] = __builtin_amdgcn_s_memrealtime(); st[3] = (xcc & 15) | ((unsigned long long)have << 8);
    }
    return;
  }
  const int r = lane >> 4, c = lane & 15;
  const size_t eo = ((size_t)bh * 2 + sp) * 2048 * D;
  auto rk = __builtin_amdgcn_make_buffer_rsrc((void*)(k + eo), 0, 2048 * D * 2, 0x00020000);
  auto rv = __builtin_amdgcn_make_buffer_rsrc((void*)(v + eo), 0, 2048 * D * 2, 0x00020000);
  const int voff = lane * 16;
  const int wo = wave * U * 1024;
  const u32x4 qv = *reinterpret_cast<const u32x4*>(q + (size_t)bh * D + 8 * c);
  SB();
  u32x4 R[NG][U];
#pragma unroll
  for (int g = 0; g < NG; ++g) {
#pragma unroll
    for (int u = 0; u < U; ++u) R[g][u] = __builtin_amdgcn_raw_buffer_load_b128(rk, voff + u * 1024, wo + g * GB, AUX_NT);
    SB();
  }
  __syncthreads();
  unsigned pt0 = 0u, ptau = 0u;
  int pn0 = 0;
  bool pseen = PACE == 0;
  auto pace = [&](int n) {
    if (PACE == 0) return;
    if (!pseen) {
      const unsigned long long cv = *ctl;
      if (__builtin_amdgcn_readfirstlane((unsigned)(cv >> 32)) == 0u) return;
      pseen = true;
      pt0 = now32(); pn0 = n;
      const int left = (int)(__builtin_amdgcn_readfirstlane((unsigned)cv) - pt0);
      ptau = left > 0 && left < 2 * PACE ? __builtin_amdgcn_readfirstlane((unsigned)((float)left * 256.0f / (float)(2 * NS - n))) : 0u;
      return;
    }
    const unsigned due = ((unsigned)(n - pn0) * ptau) >> 8;
    while (now32() - pt0 < due) __builtin_amdgcn_s_sleep(1);
  };
  float* pl = lds + L_P + wave * PW;
  float* pw = pl + r * U + (c & (U - 1));
  float mx = -INFINITY;
  auto kstep = [&](int g, int s, __amdgpu_buffer_rsrc_t rn, int so) {
    float sc[U];
#pragma unroll
    for (int u = 0; u < U; ++u) {
      float d = dot2(R[g][u][0], qv[0], 0.f);
      d = dot2(R[g][u][1], qv[1], d); d = dot2(R[g][u][2], qv[2], d); d = dot2(R[g][u][3], qv[3], d);
      sc[u] = d;
      asm volatile("" : "+v"(sc[u]));
    }
    SB();
    pace(s + NG);
#pragma unroll
    for (int u = 0; u < U; ++u) R[g][u] = __builtin_amdgcn_raw_buffer_load_b128(rn, voff + u * 1024, so, AUX_NT);
    SB();
    float t = row_allsum(sc[0]);
#pragma unroll
    for (int u = 1; u < U; ++u) { const float tu = row_allsum(sc[u]); t = (c & (U - 1)) == u ? tu : t; }
    mx = fmaxf(mx, t);
    if (c < U) pw[s * 4 * U] = t;
    asm volatile("" : "+v"(mx));
  };
  for (int s0 = 0; s0 < KM; s0 += NG) {
#pragma unroll
    for (int g = 0; g < NG; ++g) kstep(g, s0 + g, rk, wo + (s0 + g + NG) * GB);
  }
#pragma unroll
  for (int s = KM; s < NS; ++s) {
    if (s + NG < NS) kstep((s - KM) % NG, s, rk, wo + (s + NG) * GB);
    else kstep((s - KM) % NG, s, rv, wo + (s + NG - NS) * GB);
  }
  asm volatile("" ::: "memory");
  const float mw = xrow_max(row_allmax(mx)) * C2;
  float l = 0.f;
  constexpr int NV = NS * 4 * U / 256;
#pragma unroll
  for (int i = 0; i < NV; ++i) {
    f32x4 t = *reinterpret_cast<const f32x4*>(pl + (i * 64 + lane) * 4);
#pragma unroll
    for (int jx = 0; jx < 4; ++jx) { t[jx] = __builtin_amdgcn_exp2f(fmaf(t[jx], C2, -mw)); l += t[jx]; }
    *reinterpret_cast<f32x4*>(pl + (i * 64 + lane) * 4) = t;
  }
  for (int i = lane; i < NG * 4 * U; i += 64) pl[NS * 4 * U + i] = 0.f;
  asm volatile("" ::: "memory");
  l = xrow_sum(row_allsum(l));
  float acc[8];
#pragma unroll
  for (int i = 0; i < 8; ++i) acc[i] = 0.f;
  const float* pr = pl + r * U;
  float pn[U];
#pragma unroll
  for (int u = 0; u < U; ++u) pn[u] = pr[u];
  for (int j0 = 0; j0 < NS; j0 += NG) {
    if (j0 == (NS * 3 / 4 / NG) * NG && lane == 0) flag[wave] = 1u;
#pragma unroll
    for (int jj = 0; jj < NG; ++jj) {
      const int g = (NS % NG + jj) % NG;
      const int so = wo + (j0 + jj + NG) * GB;
      float p[U];
#pragma unroll
      for (int u = 0; u < U; ++u) p[u] = pn[u];
#pragma unroll
      for (int u = 0; u < U; ++u) pn[u] = pr[(j0 + jj + 1) * 4 * U + u];
#pragma unroll
      for (int u = 0; u < U; ++u) {
#pragma unroll
        for (int jx = 0; jx < 4; ++jx) {
          const unsigned w = R[g][u][jx];
          acc[2 * jx] = fmaf(p[u], __builtin_bit_cast(float, w << 16), acc[2 * jx]);
          acc[2 * jx + 1] = fmaf(p[u], __builtin_bit_cast(float, w & 0xffff0000u), acc[2 * jx + 1]);
        }
      }
      asm volatile("" : "+v"(acc[0]), "+v"(acc[1]), "+v"(acc[2]), "+v"(acc[3]), "+v"(acc[4]), "+v"(acc[5]), "+v"(acc[6]), "+v"(acc[7]));
      SB();
      if (j0 + jj + NG < NS) pace(NS + j0 + jj + NG);
#pragma unroll
      for (int u = 0; u < U; ++u) R[g][u] = __builtin_amdgcn_raw_buffer_load_b128(rv, voff + u * 1024, so, AUX_NT);
      SB();
    }
  }
#pragma unroll
  for (int i = 0; i < 8; ++i) acc[i] = xrow_sum(acc[i]);
  float* rec = lds + L_REC + wave * 132;
  if (r == 0) {
    *reinterpret_cast<f32x4*>(rec + 4 + 8 * c) = f32x4{acc[0], acc[1], acc[2], acc[3]};
    *reinterpret_cast<f32x4*>(rec + 8 + 8 * c) = f32x4{acc[4], acc[5], acc[6], acc[7]};
    if (c == 0) *reinterpret_cast<f32x2*>(rec) = f32x2{mw, l};
  }
  asm volatile("s_waitcnt lgkmcnt(0)" ::: "memory");
  if (lane == 0) flag[wave] = 2u;
}

#define STAMPS 0
extern "C" void kernel_launch(void* const* d_in, const int* in_sizes, int n_in, void* d_out, int out_size, void* d_ws,
                              size_t ws_size, hipStream_t stream) {
  attn_p<4,4,2,3500,30, STAMPS != 0><<<NWG, (4 + 1) * 64, 0, stream>>>((const uint16_t*)d_in[0], (const uint16_t*)d_in[1], (const uint16_t*)d_in[2],
                                                    (__hip_bfloat16*)d_out, (unsigned char*)d_ws);
}
```

```cpp
#include <hip/hip_runtime.h>
#include <hip/hip_bf16.h>
#include <stdint.h>

namespace {

constexpr int BH = 128, SKV = 4096, D = 128;
constexpr int NWG = BH * 2;
constexpr int NW = 4;
constexpr int U = 4;
constexpr int NG = 2;
constexpr int PACE = 3550;
constexpr int NS = 2048 / (NW * 4 * U);
constexpr int PW = (NS + NG) * 4 * U;
constexpr int GB = NW * U * 1024;
static_assert(NS % NG == 0 && (NS * 4 * U) % 256 == 0, "");
constexpr unsigned TOK_ENTRY = 0x454e5452u, TOK_READY = 0x52454459u, CLEARED = 0xffffffffu;
constexpr int MB_STRIDE = 4352;
constexpr size_t WS_MBOX = (size_t)NWG * 1024;
constexpr int AUX_NT = 2, AUX_SC1 = 16;
constexpr unsigned TMO = 20000000u;
constexpr float C2 = 0.088388347648318440f * 1.4426950408889634f;
constexpr int L_FLAG = 4, L_REC = 4 + 16, L_P = L_REC + NW * 132;

typedef unsigned u32x4 __attribute__((ext_vector_type(4)));
typedef __bf16 bf16x2 __attribute__((ext_vector_type(2)));
typedef float f32x4 __attribute__((ext_vector_type(4)));
typedef float f32x2 __attribute__((ext_vector_type(2)));
#define GAS __attribute__((address_space(1)))
#define LAS __attribute__((address_space(3)))
typedef GAS unsigned long long gu64;
#define RLX_AGENT __ATOMIC_RELAXED, __HIP_MEMORY_SCOPE_AGENT
#define SB() __builtin_amdgcn_sched_barrier(0)
#define DRAIN() asm volatile("s_waitcnt vmcnt(0)" ::: "memory")
#define CFENCE() asm volatile("" ::: "memory")

__device__ __forceinline__ float dot2(unsigned a, unsigned b, float acc) {
  return __builtin_amdgcn_fdot2_f32_bf16(__builtin_bit_cast(bf16x2, a), __builtin_bit_cast(bf16x2, b), acc, false);
}
template <int CTRL> __device__ __forceinline__ float dpp(float x) {
  return __builtin_bit_cast(float, __builtin_amdgcn_mov_dpp(__builtin_bit_cast(int, x), CTRL, 0xf, 0xf, true));
}
__device__ __forceinline__ float row_allsum(float x) {
  x += dpp<0x128>(x); x += dpp<0x124>(x); x += dpp<0x122>(x); x += dpp<0x121>(x);
  return x;
}
__device__ __forceinline__ float row_allmax(float x) {
  x = fmaxf(x, dpp<0x128>(x)); x = fmaxf(x, dpp<0x124>(x)); x = fmaxf(x, dpp<0x122>(x)); x = fmaxf(x, dpp<0x121>(x));
  return x;
}
__device__ __forceinline__ float xrow_max(float x) {
  auto s = __builtin_amdgcn_permlane16_swap(__float_as_uint(x), __float_as_uint(x), false, false);
  x = fmaxf(__uint_as_float(s[0]), __uint_as_float(s[1]));
  auto t = __builtin_amdgcn_permlane32_swap(__float_as_uint(x), __float_as_uint(x), false, false);
  return fmaxf(__uint_as_float(t[0]), __uint_as_float(t[1]));
}
__device__ __forceinline__ float xrow_sum(float x) {
  auto s = __builtin_amdgcn_permlane16_swap(__float_as_uint(x), __float_as_uint(x), false, false);
  x = __uint_as_float(s[0]) + __uint_as_float(s[1]);
  auto t = __builtin_amdgcn_permlane32_swap(__float_as_uint(x), __float_as_uint(x), false, false);
  return __uint_as_float(t[0]) + __uint_as_float(t[1]);
}
__device__ __forceinline__ unsigned now32() { return (unsigned)__builtin_amdgcn_s_memrealtime(); }
__device__ __forceinline__ bool complete(const u32x4& g) { return __all(g[1] != CLEARED && g[3] != CLEARED); }

__global__ void __launch_bounds__((NW + 1) * 64, 2)
attn_decode(const uint16_t* __restrict__ q, const uint16_t* __restrict__ k, const uint16_t* __restrict__ v,
            __hip_bfloat16* out, unsigned char* ws) {
  __shared__ __attribute__((aligned(16))) float lds[L_P + NW * PW];
  const unsigned s_me = now32();
  const int blk = blockIdx.x, x = blk & 7;
  const int bh = (x & 1) | ((blk >> 3) << 1) | ((x & 2) << 5), sp = x >> 2;
  const int tid = threadIdx.x, lane = tid & 63, wave = __builtin_amdgcn_readfirstlane(tid >> 6);
  volatile LAS unsigned long long* ctl = (volatile LAS unsigned long long*)lds;
  volatile LAS unsigned* flag = (volatile LAS unsigned*)(lds + L_FLAG);

  if (__builtin_expect(wave == NW, 0)) {
    if (lane == 0) *ctl = 0ull;
    if (lane < NW) flag[lane] = 0u;
    __syncthreads();
    auto rin = __builtin_amdgcn_make_buffer_rsrc((void*)ws, 0, (int)WS_MBOX, 0x00020000);
    const int in_me = blk * 1024, in_pt = (blk ^ 4) * 1024, lo = lane * 16;
    gu64* mb_me = (gu64*)(ws + WS_MBOX + (size_t)blk * MB_STRIDE);
    gu64* mb_pt = (gu64*)(ws + WS_MBOX + (size_t)(blk ^ 4) * MB_STRIDE);
    const unsigned long long ready = ((unsigned long long)TOK_READY << 32) | s_me;
    __builtin_amdgcn_raw_buffer_store_b128(u32x4{CLEARED, CLEARED, CLEARED, CLEARED}, rin, lo, in_me, AUX_SC1);
    if (lane == 0) __hip_atomic_store(mb_me, (unsigned long long)TOK_ENTRY << 32, RLX_AGENT);
    DRAIN();
    if (lane == 0) __hip_atomic_store(mb_pt, ready, RLX_AGENT);
    unsigned warm = __builtin_nontemporal_load((const GAS unsigned*)out + bh * (D / 2) + lane);
    asm volatile("" :: "v"(warm));
    DRAIN();
    for (bool got = false;;) {
      const unsigned long long own = __hip_atomic_load(mb_me, RLX_AGENT), par = __hip_atomic_load(mb_pt, RLX_AGENT);
      const unsigned own_t = __builtin_amdgcn_readfirstlane((unsigned)(own >> 32)), own_s = __builtin_amdgcn_readfirstlane((unsigned)own);
      if (__builtin_amdgcn_readfirstlane((unsigned)(par >> 32)) == TOK_ENTRY) {
        if (lane == 0) __hip_atomic_store(mb_pt, ready, RLX_AGENT);
        DRAIN();
      }
      if (got) break;
      if (own_t == TOK_READY) {
        got = true;
        if (lane == 0) *ctl = (1ull << 32) | (unsigned)(s_me + ((int)(own_s - s_me) >> 1) + PACE);
        continue;
      }
      if (now32() - s_me > TMO) break;
      __builtin_amdgcn_s_sleep(16);
    }
    while (__any((lane < NW ? flag[lane] : 2u) == 0u) && now32() - s_me < TMO) __builtin_amdgcn_s_sleep(32);
    u32x4 xp = {CLEARED, CLEARED, CLEARED, CLEARED};
    bool have = false;
    for (bool done = false; !done;) {
      CFENCE();
      if (!have) xp = __builtin_amdgcn_raw_buffer_load_b128(rin, lo, in_me, AUX_SC1);
      const unsigned t1 = now32();
      do {
        done = __all((lane < NW ? flag[lane] : 2u) == 2u);
        if (done) break;
        __builtin_amdgcn_s_sleep(1);
      } while (now32() - t1 < 60u);
      if (done) break;
      have = complete(xp);
      done = t1 - s_me > TMO;
    }
    CFENCE();
    const float* rec = lds + L_REC;
    float M = -INFINITY;
    f32x2 ml[NW];
#pragma unroll
    for (int w = 0; w < NW; ++w) { ml[w] = *reinterpret_cast<const f32x2*>(rec + w * 132); M = fmaxf(M, ml[w][0]); }
    float L = 0.f, o0 = 0.f, o1 = 0.f;
#pragma unroll
    for (int w = 0; w < NW; ++w) {
      const float wt = __builtin_amdgcn_exp2f(ml[w][0] - M);
      const f32x2 a = *reinterpret_cast<const f32x2*>(rec + w * 132 + 4 + 2 * lane);
      L = fmaf(wt, ml[w][1], L);
      o0 = fmaf(wt, a[0], o0);
      o1 = fmaf(wt, a[1], o1);
    }
    const float inv = 1.0f / L;
    o0 *= inv; o1 *= inv;
    unsigned tag = __float_as_uint(M + __builtin_amdgcn_logf(L));
    if (tag == CLEARED) tag = 0x7fc00000u;
    if (!have) {
      __builtin_amdgcn_raw_buffer_store_b128(u32x4{__float_as_uint(o0), tag, __float_as_uint(o1), tag}, rin, lo, in_pt, AUX_SC1);
      u32x4 xq = __builtin_amdgcn_raw_buffer_load_b128(rin, lo, in_me, AUX_SC1);
      SB();
      asm volatile("" : "+v"(xp));
      have = complete(xp);
      if (!have) {
        asm volatile("" : "+v"(xq));
        xp = xq;
        have = complete(xp);
      }
      if (!have) {
        DRAIN();
        xp = __builtin_amdgcn_raw_buffer_load_b128(rin, lo, in_me, AUX_SC1);
        have = complete(xp);
      }
    }
    if (have) {
      const float lse_me = __uint_as_float(tag), lse_pt = __uint_as_float(xp[1]);
      const float lseA = sp == 0 ? lse_me : lse_pt, lseB = sp == 0 ? lse_pt : lse_me;
      const float p0 = __uint_as_float(xp[0]), p1 = __uint_as_float(xp[2]);
      const float a0 = sp == 0 ? o0 : p0, b0 = sp == 0 ? p0 : o0, a1 = sp == 0 ? o1 : p1, b1 = sp == 0 ? p1 : o1;
      const float MM = fmaxf(lseA, lseB);
      const float wA = __builtin_amdgcn_exp2f(lseA - MM), wB = __builtin_amdgcn_exp2f(lseB - MM);
      const float iw = 1.0f / (wA + wB);
      const __hip_bfloat16 r0 = __float2bfloat16(fmaf(wA, a0, wB * b0) * iw), r1 = __float2bfloat16(fmaf(wA, a1, wB * b1) * iw);
      reinterpret_cast<unsigned*>(out)[bh * (D / 2) + lane] =
          (unsigned)__builtin_bit_cast(unsigned short, r0) | ((unsigned)__builtin_bit_cast(unsigned short, r1) << 16);
    }
    return;
  }

  const int r = lane >> 4, c = lane & 15;
  auto rk = __builtin_amdgcn_make_buffer_rsrc((void*)(k + (size_t)bh * SKV * D), 0, SKV * D * 2, 0x00020000);
  auto rv = __builtin_amdgcn_make_buffer_rsrc((void*)(v + (size_t)bh * SKV * D), 0, SKV * D * 2, 0x00020000);
  const int voff = lane * 16;
  const int wo = wave * U * 1024 + sp * GB;
  const u32x4 qv = *reinterpret_cast<const u32x4*>(q + (size_t)bh * D + 8 * c);
  SB();
  u32x4 R[NG][U];
#pragma unroll
  for (int g = 0; g < NG; ++g) {
#pragma unroll
    for (int u = 0; u < U; ++u) R[g][u] = __builtin_amdgcn_raw_buffer_load_b128(rk, voff + u * 1024, wo + g * 2 * GB, AUX_NT);
    SB();
  }
  __syncthreads();
  unsigned pt0 = 0u, ptau = 0u;
  int pn0 = 0;
  bool pseen = false;
  auto pace = [&](int n) {
    if (!pseen) {
      const unsigned long long cv = *ctl;
      if (__builtin_amdgcn_readfirstlane((unsigned)(cv >> 32)) == 0u) return;
      pseen = true;
      pt0 = now32(); pn0 = n;
      const int left = (int)(__builtin_amdgcn_readfirstlane((unsigned)cv) - pt0);
      ptau = left > 0 && left < 2 * PACE ? __builtin_amdgcn_readfirstlane((unsigned)((float)left * 256.0f / (float)(2 * NS - n))) : 0u;
      return;
    }
    const unsigned due = ((unsigned)(n - pn0) * ptau) >> 8;
    while (now32() - pt0 < due) __builtin_amdgcn_s_sleep(1);
  };
  float* pl = lds + L_P + wave * PW;
  float* pw = pl + r * U + (c & (U - 1));
  float mx = -INFINITY;
  auto kstep = [&](int g, int s, __amdgpu_buffer_rsrc_t rn, int so) {
    float sc[U];
#pragma unroll
    for (int u = 0; u < U; ++u) {
      float d = dot2(R[g][u][0], qv[0], 0.f);
      d = dot2(R[g][u][1], qv[1], d); d = dot2(R[g][u][2], qv[2], d); d = dot2(R[g][u][3], qv[3], d);
      sc[u] = d;
      asm volatile("" : "+v"(sc[u]));
    }
    SB();
    pace(s + NG);
#pragma unroll
    for (int u = 0; u < U; ++u) R[g][u] = __builtin_amdgcn_raw_buffer_load_b128(rn, voff + u * 1024, so, AUX_NT);
    SB();
    float t = row_allsum(sc[0]);
#pragma unroll
    for (int u = 1; u < U; ++u) { const float tu = row_allsum(sc[u]); t = (c & (U - 1)) == u ? tu : t; }
    mx = fmaxf(mx, t);
    if (c < U) pw[s * 4 * U] = t;
    asm volatile("" : "+v"(mx));
  };
  for (int s0 = 0; s0 < NS - NG; s0 += NG) {
#pragma unroll
    for (int g = 0; g < NG; ++g) kstep(g, s0 + g, rk, wo + (s0 + g + NG) * 2 * GB);
  }
#pragma unroll
  for (int g = 0; g < NG; ++g) kstep(g, NS - NG + g, rv, wo + g * 2 * GB);
  CFENCE();
  const float mw = xrow_max(row_allmax(mx)) * C2;
  float l = 0.f;
#pragma unroll
  for (int i = 0; i < NS * 4 * U / 256; ++i) {
    f32x4 t = *reinterpret_cast<const f32x4*>(pl + (i * 64 + lane) * 4);
#pragma unroll
    for (int j = 0; j < 4; ++j) { t[j] = __builtin_amdgcn_exp2f(fmaf(t[j], C2, -mw)); l += t[j]; }
    *reinterpret_cast<f32x4*>(pl + (i * 64 + lane) * 4) = t;
  }
  CFENCE();
  l = xrow_sum(row_allsum(l));
  float acc[8];
#pragma unroll
  for (int i = 0; i < 8; ++i) acc[i] = 0.f;
  const float* pr = pl + r * U;
  f32x4 pn = *reinterpret_cast<const f32x4*>(pr);
  for (int j0 = 0; j0 < NS; j0 += NG) {
    if (j0 == NS * 3 / 4 && lane == 0) flag[wave] = 1u;
#pragma unroll
    for (int g = 0; g < NG; ++g) {
      const f32x4 p = pn;
      pn = *reinterpret_cast<const f32x4*>(pr + (j0 + g + 1) * 4 * U);
#pragma unroll
      for (int u = 0; u < U; ++u) {
#pragma unroll
        for (int j = 0; j < 4; ++j) {
          const unsigned w = R[g][u][j];
          acc[2 * j] = fmaf(p[u], __builtin_bit_cast(float, w << 16), acc[2 * j]);
          acc[2 * j + 1] = fmaf(p[u], __builtin_bit_cast(float, w & 0xffff0000u), acc[2 * j + 1]);
        }
      }
      asm volatile("" : "+v"(acc[0]), "+v"(acc[1]), "+v"(acc[2]), "+v"(acc[3]), "+v"(acc[4]), "+v"(acc[5]), "+v"(acc[6]), "+v"(acc[7]));
      SB();
      if (j0 + g + NG < NS) pace(NS + j0 + g + NG);
#pragma unroll
      for (int u = 0; u < U; ++u) R[g][u] = __builtin_amdgcn_raw_buffer_load_b128(rv, voff + u * 1024, wo + (j0 + g + NG) * 2 * GB, AUX_NT);
      SB();
    }
  }
#pragma unroll
  for (int i = 0; i < 8; ++i) acc[i] = xrow_sum(acc[i]);
  float* rec = lds + L_REC + wave * 132;
  if (r == 0) {
    *reinterpret_cast<f32x4*>(rec + 4 + 8 * c) = f32x4{acc[0], acc[1], acc[2], acc[3]};
    *reinterpret_cast<f32x4*>(rec + 8 + 8 * c) = f32x4{acc[4], acc[5], acc[6], acc[7]};
    if (c == 0) *reinterpret_cast<f32x2*>(rec) = f32x2{mw, l};
  }
  asm volatile("s_waitcnt lgkmcnt(0)" ::: "memory");
  if (lane == 0) flag[wave] = 2u;
}

}

extern "C" void kernel_launch(void* const* d_in, const int* in_sizes, int n_in, void* d_out, int out_size, void* d_ws,
                              size_t ws_size, hipStream_t stream) {
  attn_decode<<<NWG, (NW + 1) * 64, 0, stream>>>((const uint16_t*)d_in[0], (const uint16_t*)d_in[1], (const uint16_t*)d_in[2],
                                                 (__hip_bfloat16*)d_out, (unsigned char*)d_ws);
}
```
